```python
import torch

def _softmax(x, axis=-1):
    return torch.softmax(x, dim=axis)
def _gelu(x):
    return torch.nn.functional.gelu(x, approximate="tanh")
def _silu(x):
    return torch.nn.functional.silu(x)
def _sigmoid(x):
    return torch.sigmoid(x)
def _layernorm(x, axis=-1, eps=1e-5):
    m = x.mean(dim=axis, keepdim=True)
    v = x.var(dim=axis, keepdim=True, unbiased=False)
    return (x - m) / torch.sqrt(v + eps)
def _scatter_add(tgt, idx, src, axis=0):
    return tgt.clone().index_add_(axis, idx, src)
def _segment_sum(src, seg, n):
    out = torch.zeros((n,) + src.shape[1:], dtype=src.dtype)
    return out.index_add_(0, seg, src)
def _pad(x, pw):
    flat = tuple(v for pair in reversed(pw) for v in pair)
    return torch.nn.functional.pad(x, flat)

kernel_name = 'sequential_preLN__ssm_conv_h3__dense_activation_relu__prompt'

def setup_inputs():

    _w = (lambda a: (lambda t: t.to(torch.bfloat16) if t.is_floating_point() else t)(torch.as_tensor(a)))
    def _A_setup():
        B, S, D = 1, 2048, 576
        ks = 4
        return {
            "q": _w((lambda *s: torch.randn(*s, dtype=torch.bfloat16) * 0.05)(B, S, D)),
            "k": _w((lambda *s: torch.randn(*s, dtype=torch.bfloat16) * 0.4)(B, S, D)),
            "v": _w((lambda *s: torch.randn(*s, dtype=torch.bfloat16) * 0.4)(B, S, D)),
            "shift_k": _w((lambda *s: torch.randn(*s, dtype=torch.bfloat16) * 0.4)(D, ks) * 0.5),
            "ssm_k": _w((lambda *s: torch.randn(*s, dtype=torch.bfloat16) * 0.4)(D, S) * (1.0 / S ** 0.5)),
            "ssm_D": _w((lambda *s: torch.randn(*s, dtype=torch.bfloat16) * 0.4)(D)),
        }
    _A_in = _A_setup()

    def _B_setup():
        B, S, D, Dff = 1, 2048, 576, 4608
        return {
            "x": _w((lambda *s: torch.randn(*s, dtype=torch.bfloat16) * 0.05)(B, S, D)),
            "w1": _w((lambda *s: torch.randn(*s, dtype=torch.bfloat16) * 0.05)(D, Dff)),
            "w2": _w((lambda *s: torch.randn(*s, dtype=torch.bfloat16) * 0.05)(Dff, D)),
        }
    _B_in = _B_setup()

    return {
        "A_q": _A_in["q"],
        "A_k": _A_in["k"],
        "A_v": _A_in["v"],
        "A_shift_k": _A_in["shift_k"],
        "A_ssm_k": _A_in["ssm_k"],
        "A_ssm_D": _A_in["ssm_D"],
        "B_x": _B_in["x"],
        "B_w1": _B_in["w1"],
        "B_w2": _B_in["w2"],
    }

def reference(A_q, A_k, A_v, A_shift_k, A_ssm_k, A_ssm_D, B_x, B_w1, B_w2):
    def _A_ref(q, k, v, shift_k, ssm_k, ssm_D):
        B, S, D = q.shape
        ks = shift_k.shape[-1]
        k_pad = _pad(k, ((0, 0), (ks - 1, 0), (0, 0)))
        k_shift = torch.zeros_like(k)
        for t in range(ks):
            k_shift = k_shift + k_pad[:, t:t + S, :] * shift_k[:, ks - 1 - t][None, None, :]
        kv = k_shift * v
        L = 2 * S
        kv_pad = _pad(kv, ((0, 0), (0, S), (0, 0)))
        kf_pad = _pad(ssm_k, ((0, 0), (0, S)))
        kv_f = (lambda x, axes=(-2,-1): torch.fft.rfft2(x.float(), dim=axes))(kv_pad[:, None, :, :], axes=(1, 2))
        kf_f = (lambda x, axes=(-2,-1): torch.fft.rfft2(x.float(), dim=axes))(kf_pad[:, None, :], axes=(1, 2))
        kf_f = (lambda x, *d: x.permute(*d))(kf_f, 1, 2, 0)
        y_f = kv_f * kf_f[None, :, :, :]
        y = (lambda x, s, axes=(-2,-1): torch.fft.irfft2(x, s=s, dim=axes).to(torch.bfloat16))(y_f, (1, L), axes=(1, 2))[:, 0, :S, :]
        y = y + kv * ssm_D[None, None, :]
        return q * y

    def _B_ref(x, w1, w2):
        h = torch.einsum("bsd,df->bsf", x, w1)
        zero = torch.zeros_like(h)
        h = torch.maximum(h, zero)
        return torch.einsum("bsf,fd->bsd", h, w2)

    x = A_q
    h = x + _A_ref(_layernorm(x, axis=-1), A_k, A_v, A_shift_k, A_ssm_k, A_ssm_D)
    h2 = h + _B_ref(_layernorm(h, axis=-1), B_w1, B_w2)
    return h2
```

```cpp
#include <hip/hip_runtime.h>
#include <hip/hip_bf16.h>
#include <stdint.h>

typedef unsigned short u16;
using bf16x8 = __attribute__((ext_vector_type(8))) short;
using f32x4 = __attribute__((ext_vector_type(4))) float;

constexpr int S = 2048, D = 576, F = 4608;

__device__ __forceinline__ float bf2f(u16 x) { return __uint_as_float(((unsigned)x) << 16); }
__device__ __forceinline__ u16 f2bf(float f) { __hip_bfloat16 b = __float2bfloat16(f); return *reinterpret_cast<u16*>(&b); }
__device__ __forceinline__ float rbf(float f) { return bf2f(f2bf(f)); }

__global__ void k_kv(const u16* __restrict__ k, const u16* __restrict__ v, const u16* __restrict__ shk, u16* __restrict__ kvT) {
    int idx = blockIdx.x * blockDim.x + threadIdx.x;
    if (idx >= S * D) return;
    int s = idx / D, d = idx % D;
    float acc = 0.f;
    for (int t = 0; t < 4; ++t) {
        int ss = s + t - 3;
        float kk = ss >= 0 ? bf2f(k[ss * D + d]) : 0.f;
        float p = rbf(kk * bf2f(shk[d * 4 + 3 - t]));
        acc = rbf(acc + p);
    }
    kvT[d * S + s] = f2bf(acc * bf2f(v[idx]));
}

__global__ void k_conv(const u16* __restrict__ kvT, const u16* __restrict__ filt, const u16* __restrict__ Dv, u16* __restrict__ yp) {
    __shared__ float xs[S];
    __shared__ float fs[S];
    int d = blockIdx.x;
    for (int i = threadIdx.x; i < S; i += blockDim.x) { xs[i] = bf2f(kvT[d * S + i]); fs[i] = bf2f(filt[d * S + i]); }
    __syncthreads();
    float dd = bf2f(Dv[d]);
    for (int s = threadIdx.x; s < S; s += blockDim.x) {
        float acc = 0.f;
        for (int j = 0; j <= s; ++j) acc += fs[j] * xs[s - j];
        float y = rbf(acc);
        y = rbf(y + rbf(xs[s] * dd));
        yp[s * D + d] = f2bf(y);
    }
}

__device__ __forceinline__ float wsum(float x) {
    for (int o = 32; o > 0; o >>= 1) x += __shfl_xor(x, o);
    return x;
}

__global__ void k_row(const u16* __restrict__ q, const u16* __restrict__ yp, u16* __restrict__ h, u16* __restrict__ hn) {
    int row = blockIdx.x * 4 + (threadIdx.x >> 6);
    int lane = threadIdx.x & 63;
    float x[9], y[9];
    float sum = 0.f;
    for (int i = 0; i < 9; ++i) { x[i] = bf2f(q[row * D + lane + 64 * i]); y[i] = bf2f(yp[row * D + lane + 64 * i]); sum += x[i]; }
    sum = wsum(sum);
    float mean = sum / D;
    float vs = 0.f;
    for (int i = 0; i < 9; ++i) { float t = x[i] - mean; vs += t * t; }
    vs = wsum(vs) / D;
    float m = rbf(mean), den = rbf(sqrtf(rbf(rbf(vs) + 1e-5f)));
    float hh[9];
    sum = 0.f;
    for (int i = 0; i < 9; ++i) {
        float n = rbf(rbf(x[i] - m) / den);
        float a = rbf(n * y[i]);
        hh[i] = rbf(x[i] + a);
        h[row * D + lane + 64 * i] = f2bf(hh[i]);
        sum += hh[i];
    }
    sum = wsum(sum);
    mean = sum / D;
    vs = 0.f;
    for (int i = 0; i < 9; ++i) { float t = hh[i] - mean; vs += t * t; }
    vs = wsum(vs) / D;
    m = rbf(mean); den = rbf(sqrtf(rbf(rbf(vs) + 1e-5f)));
    for (int i = 0; i < 9; ++i) hn[row * D + lane + 64 * i] = f2bf(rbf(hh[i] - m) / den);
}

template <int MODE>
__global__ void k_gemm(const u16* __restrict__ A, const u16* __restrict__ B, const u16* __restrict__ res, u16* __restrict__ C, int M, int N, int K) {
    int wave = blockIdx.x * 4 + (threadIdx.x >> 6);
    int lane = threadIdx.x & 63;
    int nt = N / 16;
    int tm = wave / nt, tn = wave % nt;
    int fr = lane & 15, fq = lane >> 4;
    f32x4 acc = {0, 0, 0, 0};
    for (int k0 = 0; k0 < K; k0 += 32) {
        bf16x8 a = *reinterpret_cast<const bf16x8*>(A + (size_t)(tm * 16 + fr) * K + k0 + fq * 8);
        bf16x8 b;
        for (int j = 0; j < 8; ++j) b[j] = (short)B[(size_t)(k0 + fq * 8 + j) * N + tn * 16 + fr];
        acc = __builtin_amdgcn_mfma_f32_16x16x32_bf16(a, b, acc, 0, 0, 0);
    }
    for (int j = 0; j < 4; ++j) {
        int r = tm * 16 + fq * 4 + j, c = tn * 16 + fr;
        if (MODE == 0) C[(size_t)r * N + c] = f2bf(fmaxf(rbf(acc[j]), 0.f));
        else C[(size_t)r * N + c] = f2bf(bf2f(res[(size_t)r * N + c]) + rbf(acc[j]));
    }
}

extern "C" void kernel_launch(void* const* d_in, const int* in_sizes, int n_in,
                              void* d_out, int out_size, void* d_ws, size_t ws_size,
                              hipStream_t stream) {
    const u16* q = (const u16*)d_in[0];
    const u16* k = (const u16*)d_in[1];
    const u16* v = (const u16*)d_in[2];
    const u16* shk = (const u16*)d_in[3];
    const u16* filt = (const u16*)d_in[4];
    const u16* Dv = (const u16*)d_in[5];
    const u16* w1 = (const u16*)d_in[7];
    const u16* w2 = (const u16*)d_in[8];
    u16* out = (u16*)d_out;
    char* ws = (char*)d_ws;
    u16* kvT = (u16*)(ws);
    u16* yp = (u16*)(ws + (4 << 20));
    u16* h = (u16*)(ws + (8 << 20));
    u16* hn = (u16*)(ws + (12 << 20));
    u16* H = (u16*)(ws + (16 << 20));
    k_kv<<<(S * D + 255) / 256, 256, 0, stream>>>(k, v, shk, kvT);
    k_conv<<<D, 256, 0, stream>>>(kvT, filt, Dv, yp);
    k_row<<<S / 4, 256, 0, stream>>>(q, yp, h, hn);
    k_gemm<0><<<(S / 16) * (F / 16) / 4, 256, 0, stream>>>(hn, w1, nullptr, H, S, F, D);
    k_gemm<1><<<(S / 16) * (D / 16) / 4, 256, 0, stream>>>(H, w2, h, out, S, D, F);
}
```

```cpp
#include <hip/hip_runtime.h>
#include <hip/hip_bf16.h>
#include <stdint.h>

typedef unsigned short u16;
using bf16x8 = __attribute__((ext_vector_type(8))) short;
using s16x4 = __attribute__((ext_vector_type(4))) short;
using f32x4 = __attribute__((ext_vector_type(4))) float;
using u32x4 = __attribute__((ext_vector_type(4))) unsigned;
using u32x2 = __attribute__((ext_vector_type(2))) unsigned;
typedef __bf16 bf2v __attribute__((ext_vector_type(2)));
typedef float f2v __attribute__((ext_vector_type(2)));
#define LAS __attribute__((address_space(3)))
#define GAS __attribute__((address_space(1)))

constexpr int S = 2048, D = 576, F = 4608;
constexpr int CST = 145;
constexpr int FSX = 2;
constexpr int NREP = 16;
constexpr int SH0 = 8, SHH = 8;
__device__ __forceinline__ float bf2f(u16 x) { return __uint_as_float(((unsigned)x) << 16); }
__device__ __forceinline__ unsigned cvt2(float a, float b) { f2v x = {a, b}; return __builtin_bit_cast(unsigned, __builtin_convertvector(x, bf2v)); }
__device__ __forceinline__ float rbf(float f) { return __uint_as_float(cvt2(f, f) & 0xffff0000u); }
#define CODE_RSRC(name, sym) \
    __amdgpu_buffer_rsrc_t name; { unsigned lo_, hi_, elo_; \
    asm volatile("s_getpc_b64 s[20:21]\n\ts_add_u32 %0, s20, " sym "@rel32@lo+4\n\ts_addc_u32 %1, s21, " sym "@rel32@hi+12\n\t" \
                 "s_getpc_b64 s[20:21]\n\ts_add_u32 %2, s20, code_end_lbl@rel32@lo+4" : "=&s"(lo_), "=&s"(hi_), "=&s"(elo_) :: "s20", "s21", "scc"); \
    name = __builtin_amdgcn_make_buffer_rsrc((void*)(((unsigned long long)hi_ << 32) | lo_), 0, (int)(elo_ - lo_), 0x00020000); }

__device__ __forceinline__ void bdma16s(__amdgpu_buffer_rsrc_t rs, unsigned voff, unsigned soff, unsigned lds_dst) {
    asm volatile("s_mov_b32 m0, %1\n\ts_nop 0\n\tbuffer_load_dwordx4 %0, %2, %3 offen lds" :: "v"(voff), "s"(lds_dst), "s"(rs), "s"(soff) : "memory");
}
__device__ __forceinline__ void bdma16n(__amdgpu_buffer_rsrc_t rs, unsigned voff, unsigned soff, unsigned lds_dst) {
    asm volatile("s_mov_b32 m0, %1\n\ts_nop 0\n\tbuffer_load_dwordx4 %0, %2, %3 offen nt lds" :: "v"(voff), "s"(lds_dst), "s"(rs), "s"(soff) : "memory");
}
__device__ __forceinline__ void bdma4s(__amdgpu_buffer_rsrc_t rs, unsigned voff, unsigned lds_dst) {
    asm volatile("s_mov_b32 m0, %1\n\ts_nop 0\n\tbuffer_load_dword %0, %2, 0 offen lds" :: "v"(voff), "s"(lds_dst), "s"(rs) : "memory");
}
__device__ __forceinline__ bf16x8 tr_frag(LAS char* p, int second) {
    s16x4 a = __builtin_amdgcn_ds_read_tr16_b64_v4i16((LAS s16x4*)p);
    s16x4 b = __builtin_amdgcn_ds_read_tr16_b64_v4i16((LAS s16x4*)(p + second));
    return __builtin_shufflevector(a, b, 0, 1, 2, 3, 4, 5, 6, 7);
}
template <int CTRL> __device__ __forceinline__ float dpp_add(float x) {
    return x + __builtin_bit_cast(float, __builtin_amdgcn_update_dpp(0, __builtin_bit_cast(int, x), CTRL, 0xf, 0xf, true));
}
__device__ __forceinline__ float wave_sum(float x) {
    x = dpp_add<0xB1>(x); x = dpp_add<0x4E>(x); x = dpp_add<0x141>(x); x = dpp_add<0x140>(x);
    const int v = __builtin_bit_cast(int, x);
    return (__builtin_bit_cast(float, __builtin_amdgcn_readlane(v, 0)) + __builtin_bit_cast(float, __builtin_amdgcn_readlane(v, 16))) +
           (__builtin_bit_cast(float, __builtin_amdgcn_readlane(v, 32)) + __builtin_bit_cast(float, __builtin_amdgcn_readlane(v, 48)));
}
#define RAWBAR() do { asm volatile("s_waitcnt lgkmcnt(0)" ::: "memory"); __builtin_amdgcn_s_barrier(); asm volatile("" ::: "memory"); } while (0)

constexpr int LWS = 21792, UGB = 4 * LWS + 128;
constexpr int HT_OFF = 0, H_OFF = 73728, ST_OFF = 147456, CS_OFF = ST_OFF + 512, RS_OFF = CS_OFF + 4096, PREP_OFF = 125440, LDS_BYTES = PREP_OFF + 32064 + 512;
extern "C" __global__ __launch_bounds__(256, 1) void kk_ffn(const u16* q, const u16* k, const u16* v, const u16* shk, const u16* Dv, const u16* filt, char* XT, char* QT, const u16* w1, const u16* w2, u16* hT, u32x4* wstream, float* csum, char* part, unsigned* cnt, u16* out) {
    __shared__ __attribute__((aligned(16))) char smem[LDS_BYTES];
    const unsigned L = (unsigned)(size_t)(LAS char*)smem;
    if ((blockIdx.x >> 4) == 15) {
        CODE_RSRC(rcode, "kk_ffn");
        bdma4s(rcode, (((blockIdx.x >> 3) & 1) * 256 + threadIdx.x) * 128, L + 122880);
    }
    __builtin_amdgcn_sched_barrier(0);
    const int tid = threadIdx.x, lane = tid & 63;
    const int w = __builtin_amdgcn_readfirstlane(tid >> 6);
    const int x = blockIdx.x & 7, bi = blockIdx.x >> 3;
    constexpr int XG = 8 / FSX;
    const int fs = (x % XG) * FSX + (bi % FSX);
    const int mtile = (x / XG) * (32 / FSX) + bi / FSX;
    const int r = lane & 15, g = lane >> 4, q4 = r >> 2, p4 = r & 3;
    const unsigned voff = lane * 16;
    const int sigB = ((q4 >> 1) & 1) | ((g & 1) << 1);

    unsigned tag;
    u32x4 FA[4], FB[4];
    unsigned dch = 0;
    auto fstage = [&]() {
            LAS char* LP = (LAS char*)smem;
            LAS char* fw = LP + w * LWS + 64 + lane * 16;
#define AB(hi, lo_) __builtin_amdgcn_alignbit(hi, lo_, 16)
#pragma unroll
            for (int i = 0; i < 4; ++i) {
                const u32x4 A = FA[i], B = FB[i];
                *(LAS u32x4*)(fw + i * 1024) = u32x4{AB(A[1], A[0]), AB(A[2], A[1]), AB(A[3], A[2]), AB(B[2], A[3])};
                *(LAS u32x4*)(fw + i * 1024 + 4160) = u32x4{A[1], A[2], A[3], B[2]};
                *(LAS u32x4*)(fw + i * 1024 + 2 * 4160) = u32x4{AB(A[2], A[1]), AB(A[3], A[2]), AB(B[2], A[3]), AB(B[3], B[2])};
                *(LAS u32x4*)(fw + i * 1024 + 3 * 4160) = B;
            }
            *(LAS u32x4*)(LP + w * LWS + 16640 + g * 1280 + r * 16) = u32x4{0u, 0u, 0u, 0u};
            const unsigned long long V = ((unsigned long long)(unsigned)__builtin_amdgcn_readfirstlane(FA[0][1]) << 32) | (unsigned)__builtin_amdgcn_readfirstlane(FA[0][0]);
            const int i0 = 2 * r + g - 31;
            *(LAS unsigned*)(LP + w * LWS + g * 4160 + r * 4) = i0 >= 0 ? (unsigned)(V >> (16 * i0)) : i0 == -1 ? (unsigned)(V << 16) : 0u;
        };
    auto w2dma = [&]() {
            const int xa = blockIdx.x & 7, li = blockIdx.x >> 3;
            {
                unsigned wv[6];
#pragma unroll
                for (int i = 0; i < 6; ++i) { const int Pp = i * 64 + lane; wv[i] = (unsigned)((Pp / 12) * (D * 2) + (Pp % 12) * 16); }
#pragma unroll
                for (int n = 0; n < 2; ++n) {
                    const int u2 = (li - 18) * 8 + n * 4 + w, js = u2 / 6, cq = u2 % 6;
                    const __amdgpu_buffer_rsrc_t r2 = __builtin_amdgcn_make_buffer_rsrc((void*)w2, 0, u2 < 108 ? D * F * 2 : 0, 0x00020000);
                    const unsigned wsf = (unsigned)((576 * xa + 32 * js) * (D * 2) + 192 * cq);
#pragma unroll
                    for (int i = 0; i < 6; ++i) bdma16s(r2, wv[i], wsf, L + 49152 + (n * 4 + w) * 6144 + i * 1024);
                }
            }
    };
    auto arrive = [&](int ctr, unsigned n, int rel, int word) {
        unsigned old = 0;
        if (lane == 0) old = __hip_atomic_fetch_add((GAS unsigned*)cnt + 8192 + ctr * 32, 1u, __ATOMIC_RELAXED, __HIP_MEMORY_SCOPE_AGENT);
        if ((unsigned)__builtin_amdgcn_readfirstlane(old) == n - 1 && lane < NREP)
            __hip_atomic_store((GAS unsigned*)cnt + 20480 + rel * 2048 + lane * 32 + word, tag, __ATOMIC_RELAXED, __HIP_MEMORY_SCOPE_AGENT);
    };
    {
        LAS char* P = (LAS char*)smem + PREP_OFF;
        const unsigned Pa = L + PREP_OFF;
        const int c = blockIdx.x;
        GAS unsigned* ep = (GAS unsigned*)cnt + 16384;
        unsigned e0;
#define EPOCH() asm volatile("s_load_dword %0, %1, 0x0\n\ts_waitcnt lgkmcnt(0)" : "=s"(e0) : "s"(ep) : "memory")
        if (c == 0 && w == 3) {
            if (lane < 48) __hip_atomic_store((GAS unsigned*)cnt + 8192 + lane * 32, 0u, __ATOMIC_RELAXED, __HIP_MEMORY_SCOPE_AGENT);
            EPOCH();
            asm volatile("s_waitcnt vmcnt(0)" ::: "memory");
            if (lane < 16) __hip_atomic_store((GAS unsigned*)cnt + 20480 + lane * 32, e0 * 0x9E3779B1u + 0x7F4A7C15u, __ATOMIC_RELAXED, __HIP_MEMORY_SCOPE_AGENT);
        }
        const __amdgpu_buffer_rsrc_t rq = __builtin_amdgcn_make_buffer_rsrc((void*)q, 0, S * D * 2, 0x00020000);
        const __amdgpu_buffer_rsrc_t rk = __builtin_amdgcn_make_buffer_rsrc((void*)k, 0, S * D * 2, 0x00020000);
        const __amdgpu_buffer_rsrc_t rv = __builtin_amdgcn_make_buffer_rsrc((void*)v, 0, S * D * 2, 0x00020000);
        const unsigned off = c * 9216 + lane * 16;
        const bool cv = (c >> 3) < 18;
        const __amdgpu_buffer_rsrc_t rf = __builtin_amdgcn_make_buffer_rsrc((void*)filt, 0, cv ? S * D * 2 : 0, 0x00020000);
        const int fo = (((c & 7) * 18 + (c >> 3)) * 4 + w) * 4096 + lane * 16;
#define F_LOADS() do { { dch = Dv[cv ? ((c & 7) * 18 + (c >> 3)) * 4 + w : 0]; _Pragma("unroll") for (int i = 0; i < 4; ++i) { FA[i] = __builtin_amdgcn_raw_buffer_load_b128(rf, fo + i * 1024, 0, 0); FB[i] = __builtin_amdgcn_raw_buffer_load_b128(rf, fo + i * 1024 + 8, 0, 0); } } \
                       __builtin_amdgcn_sched_barrier(0); } while (0)
#define INIT_SEEN() for (int spin = 0; spin < (1 << 17) && inif != tag; ++spin) inif = __hip_atomic_load((GAS unsigned*)cnt + 20480 + (c & 15) * 32, __ATOMIC_RELAXED, __HIP_MEMORY_SCOPE_AGENT)
        auto second = [&]() {
            unsigned o = 0;
            if (lane == 0) o = __hip_atomic_fetch_add((LAS unsigned*)(P + 31808 + 252), 1u, __ATOMIC_RELAXED, __HIP_MEMORY_SCOPE_WORKGROUP);
            if (o == 3) __hip_atomic_fetch_add((GAS unsigned*)cnt + 8192 + (16 + (c & 3)) * 32, 1u, __ATOMIC_RELAXED, __HIP_MEMORY_SCOPE_AGENT);
        };
        if (w == 3) {
#pragma unroll
            for (int i = 0; i < 9; ++i) bdma16n(rq, off, i * 1024, Pa + i * 1024);
            if (lane < 32) __hip_atomic_store((GAS unsigned*)cnt + c * 32 + lane, 0u, __ATOMIC_RELAXED, __HIP_MEMORY_SCOPE_AGENT);
            if (lane < 3) *(LAS unsigned*)(P + 31808 + 244 + lane * 4) = 0u;
            auto w1dma = [&](int n) {
                const int xa = c & 7, li = c >> 3, krow = lane >> 3, sl = lane & 7;
                const unsigned src0 = (unsigned)(krow * (F * 2) + (((sl >> 1) ^ ((krow >> 1) & 1)) * 32) + (sl & 1) * 16), src1 = src0 ^ 64u;
                const int u1 = 2 * (li - 18) + n, cgp = u1 % 9, kt = u1 / 9;
                const __amdgpu_buffer_rsrc_t r1 = __builtin_amdgcn_make_buffer_rsrc((void*)w1, 0, u1 < 27 ? D * F * 2 : 0, 0x00020000);
                const unsigned sb = (unsigned)(192 * kt * (F * 2) + (576 * xa + 64 * cgp) * 2);
#pragma unroll 1
                for (int i = 0; i < 12; ++i) {
                    bdma16n(r1, src0, sb + i * (16 * F * 2), L + n * 24576 + i * 2048);
                    bdma16n(r1, src1, sb + i * (16 * F * 2) + 8 * F * 2, L + n * 24576 + i * 2048 + 1024);
                }
            };
            if (!cv) w1dma(0);
            EPOCH();
            if (!cv) asm volatile("s_waitcnt vmcnt(24)" ::: "memory"); else asm volatile("s_waitcnt vmcnt(0)" ::: "memory");
            RAWBAR();
            if (!cv) w1dma(1);
            tag = e0 * 0x9E3779B1u + 0x7F4A7C15u;
            unsigned inif = __hip_atomic_load((GAS unsigned*)cnt + 20480 + (c & 15) * 32, __ATOMIC_RELAXED, __HIP_MEMORY_SCOPE_AGENT);
            if (cv) F_LOADS();
            float a[8][10], s[8], vs[8];
#pragma unroll
            for (int rr = 0; rr < 8; ++rr) {
                const LAS unsigned* row = (const LAS unsigned*)(P + rr * 1152);
#pragma unroll
                for (int j = 0; j < 5; ++j) {
                    const unsigned u = (j < 4 || lane < 32) ? row[lane + 64 * j] : 0u;
                    a[rr][2 * j] = __uint_as_float(u << 16); a[rr][2 * j + 1] = __uint_as_float(u & 0xffff0000u);
                }
                s[rr] = 0.f;
#pragma unroll
                for (int j = 0; j < 10; ++j) s[rr] += a[rr][j];
            }
#pragma unroll
            for (int rr = 0; rr < 8; ++rr) s[rr] = wave_sum(s[rr]) * (1.f / D);
#pragma unroll
            for (int rr = 0; rr < 8; ++rr) {
                vs[rr] = 0.f;
#pragma unroll
                for (int j = 0; j < 10; ++j) { const float t = a[rr][j] - s[rr]; vs[rr] += (j < 8 || lane < 32) ? t * t : 0.f; }
            }
            float mm = 0.f, vv = 0.f;
#pragma unroll
            for (int rr = 0; rr < 8; ++rr) { const float t = wave_sum(vs[rr]) * (1.f / D); if ((lane & 7) == rr) { mm = s[rr]; vv = t; } }
            if (lane < 16) {
                const float ri = __builtin_amdgcn_rcpf(rbf(__builtin_amdgcn_sqrtf(rbf(rbf(vv) + 1e-5f))));
                __hip_atomic_store((GAS float*)(QT + 128 * CST * 128) + c * 16 + lane, lane < 8 ? ri : -rbf(mm) * ri, __ATOMIC_RELAXED, __HIP_MEMORY_SCOPE_AGENT);
            }
            asm volatile("s_waitcnt vmcnt(0)" ::: "memory");
            INIT_SEEN();
            second();
        } else {
#pragma unroll
            for (int i = 0; i < 4; ++i) bdma16s(rk, off - 3456 + (w + 3 * i) * 1024, 0, Pa + 18432 + (w + 3 * i) * 1024);
#pragma unroll
            for (int i = 0; i < 3; ++i) bdma16n(rv, off, (w + 3 * i) * 1024, Pa + 9216 + (w + 3 * i) * 1024);
            if (w == 0) bdma16s(rk, off - 3456 + 12288, 0, Pa + 18432 + 12288);
            const int chs[3] = {tid, tid + 192, tid + 384};
            u32x2 sw[3];
#pragma unroll
            for (int h = 0; h < 3; ++h) sw[h] = *reinterpret_cast<const u32x2*>(shk + chs[h] * 4);
            __builtin_amdgcn_sched_barrier(0);
            F_LOADS();
            EPOCH();
            asm volatile("s_waitcnt vmcnt(9)" : "+v"(sw[0]), "+v"(sw[1]), "+v"(sw[2]) :: "memory");
            __builtin_amdgcn_s_barrier();
            asm volatile("" ::: "memory");
            tag = e0 * 0x9E3779B1u + 0x7F4A7C15u;
            unsigned inif = __hip_atomic_load((GAS unsigned*)cnt + 20480 + (c & 15) * 32, __ATOMIC_RELAXED, __HIP_MEMORY_SCOPE_AGENT);
            const __amdgpu_buffer_rsrc_t rx = __builtin_amdgcn_make_buffer_rsrc((void*)XT, 0, 128 * CST * 128, 0x00020000);
            const __amdgpu_buffer_rsrc_t ru = __builtin_amdgcn_make_buffer_rsrc((void*)QT, 0, 128 * CST * 128, 0x00020000);
            const unsigned lo = (tid >> 2) * 128 + (tid & 3) * 16;
            float kv[3][8];
#pragma unroll
            for (int h = 0; h < 3; ++h) {
                const LAS u16* tv = (const LAS u16*)(P + 9216) + chs[h];
                const LAS u16* tk = (const LAS u16*)(P + 18432) + chs[h];
                const float w0 = __uint_as_float(sw[h][0] << 16), w1f = __uint_as_float(sw[h][0] & 0xffff0000u);
                const float w2f = __uint_as_float(sw[h][1] << 16), w3 = __uint_as_float(sw[h][1] & 0xffff0000u);
                float kk[11];
#pragma unroll
                for (int rr = 0; rr < 11; ++rr) kk[rr] = bf2f(tk[rr * D]);
#pragma unroll
                for (int rr = 0; rr < 8; ++rr) kv[h][rr] = (kk[rr] * w3 + kk[rr + 1] * w2f + kk[rr + 2] * w1f + kk[rr + 3] * w0) * bf2f(tv[rr * D]);
                __builtin_amdgcn_raw_buffer_store_b128(u32x4{cvt2(kv[h][7], kv[h][6]), cvt2(kv[h][5], kv[h][4]), cvt2(kv[h][3], kv[h][2]), cvt2(kv[h][1], kv[h][0])}, rx,
                                                       lo + h * 6144 + (c >> 1) * (CST * 128) + (c & 1) * 64, 0, 16);
            }
            __builtin_amdgcn_sched_barrier(0);
#pragma unroll
            for (int h = 0; h < 3; ++h) {
                const LAS u16* tq = (const LAS u16*)P + chs[h];
                unsigned pq[4];
#pragma unroll
                for (int j = 0; j < 4; ++j) pq[j] = (unsigned)tq[2 * j * D] | ((unsigned)tq[(2 * j + 1) * D] << 16);
                __builtin_amdgcn_raw_buffer_store_b128(u32x4{pq[0], pq[1], pq[2], pq[3]}, ru, lo + h * 6144 + (c >> 1) * (CST * 128) + (c & 1) * 64, 0, 16);
            }
            __builtin_amdgcn_sched_barrier(0);
            asm volatile("s_waitcnt vmcnt(3)" : "+v"(inif) :: "memory");
            INIT_SEEN();
            unsigned o = 0;
            if (lane == 0) o = __hip_atomic_fetch_add((LAS unsigned*)(P + 31808 + 248), 1u, __ATOMIC_RELAXED, __HIP_MEMORY_SCOPE_WORKGROUP);
            if (__builtin_amdgcn_readfirstlane(o) == 2) arrive(c & (SH0 - 1), 256 / SH0, 1, c & (SH0 - 1));
            else asm volatile("s_waitcnt vmcnt(0)" ::: "memory");
            second();
            if (cv) fstage();
        }
        tag = __builtin_amdgcn_readfirstlane(tag);
        asm volatile("" ::: "memory");
    }
    auto gate0 = [&]() {
        if (w == 3) {
            GAS unsigned* rel = (GAS unsigned*)cnt + 20480 + 2048 + (blockIdx.x & (NREP - 1)) * 32 + (lane & (SH0 - 1));
            for (int spin = 0; spin < (1 << 17); ++spin) {
                const unsigned f = __hip_atomic_load(rel, __ATOMIC_RELAXED, __HIP_MEMORY_SCOPE_AGENT);
                if (__all(f == tag)) break;
                __builtin_amdgcn_s_sleep(5);
            }
            __builtin_amdgcn_fence(__ATOMIC_ACQUIRE, "wavefront");
            const __amdgpu_buffer_rsrc_t rx = __builtin_amdgcn_make_buffer_rsrc((void*)XT, 0, 128 * CST * 128, 0x00020000);
            const int cg = (blockIdx.x & 7) * 18 + (blockIdx.x >> 3);
            u32x4 xr[16];
#pragma unroll
            for (int i = 0; i < 16; ++i) xr[i] = __builtin_amdgcn_raw_buffer_load_b128(rx, (lane >> 3) * (CST * 128) + (lane & 7) * 16, (8 * i * CST + cg) * 128, 16);
            __builtin_amdgcn_sched_barrier(0);
            if (blockIdx.x == 0 && lane == 0) __hip_atomic_store((GAS unsigned*)cnt + 16384, tag, __ATOMIC_RELAXED, __HIP_MEMORY_SCOPE_AGENT);
            fstage();
            __builtin_amdgcn_sched_barrier(0);
            LAS char* xw = (LAS char*)smem + (lane & 3) * LWS + 16640 + (2 * ((lane >> 3) & 1) + ((lane >> 2) & 1)) * 1280 + (16 + (lane >> 4)) * 16;
#pragma unroll
            for (int i = 0; i < 16; ++i) *(LAS u32x4*)(xw + 64 * i) = xr[i];
        }
    };
    const unsigned gwo = (20480 + 3 * 2048 + (blockIdx.x & (NREP - 1)) * 32 + fs) * 4, gho = (20480 + 2 * 2048 + (blockIdx.x & (NREP - 1)) * 32 + (lane & (SHH - 1))) * 4;
    const char* pG = (const char*)cnt;
    unsigned wok = 0;

    {
        LAS char* LP = (LAS char*)smem;
        const int xa = blockIdx.x & 7, li = blockIdx.x >> 3;
        if (li < 18) {
            const int cg = xa * 18 + li, ch = cg * 4 + w;
            const unsigned Lw = L + w * LWS;
            const unsigned vx = (lane >> 3) * (CST * 128) + (lane & 7) * 16;
            const unsigned bA = Lw + (r & 3) * 4160 + ((r >> 2) + g) * 16;
            const unsigned bX = Lw + 16640 + (3 - g) * 1280 + (1 + r) * 16;
            const unsigned uU = L + UGB + g * 1280 + w * 288 + r * 16, sU = L + UGB + 20480 + g * 256 + r * 16;
            const unsigned uw = L + UGB + w * 5120 + (2 * ((lane >> 3) & 1) + ((lane >> 2) & 1)) * 1280 + (lane & 3) * 288 + (lane >> 4) * 16;
            const unsigned sw = L + UGB + 20480 + w * 4352 + (lane & 3) * 1056 + ((lane >> 2) & 3) * 256 + (lane >> 4) * 16, sv = 128 * CST * 128 + w * 4096 + lane * 16;
            const unsigned us = (32 * w * CST + cg) * 128;
            const unsigned vo = (r >> 1) * 73728 + (r & 1) * 64 + g * 16;
            unsigned wfl;
            const unsigned dv = (g == 3 - (r >> 2)) ? ((r & 1) ? dch : dch << 16) : 0u;
            const u32x4 ds0 = {0u, 0u, (r & 2) ? dv : 0u, (r & 2) ? 0u : dv}, ds1 = {(r & 2) ? dv : 0u, (r & 2) ? 0u : dv, 0u, 0u};
            const unsigned guo = (8192 + (16 + (lane & 3)) * 32) * 4;
            gate0();
            asm volatile(
    "s_mov_b64 s[44:45], %[pO]\n\t"
    "s_mov_b32 s46, 2359296\n\t"
    "s_mov_b32 s47, 0x20000\n\t"
    "s_mov_b64 s[48:49], %[pU]\n\t"
    "s_mov_b32 s50, 2392064\n\t"
    "s_mov_b32 s51, 0x20000\n\t"
    "s_lshl_b32 s55, %[ch], 12\n\t"
    "s_lshl_b32 s53, %[ch], 7\n\t"
    "s_waitcnt lgkmcnt(0)\n\t"
    "s_barrier\n\t"
    "ds_read_b128 v[32:35], %[bA] offset:0\n\t"
    "ds_read_b64 v[36:37], %[bA] offset:16\n\t"
    "ds_read_b128 a[192:195], %[bX] offset:240\n\t"
    "ds_read_b128 a[128:131], %[bX] offset:496\n\t"
    "ds_read_b128 a[64:67], %[bX] offset:752\n\t"
    "ds_read_b128 a[0:3], %[bX] offset:1008\n\t"
    "ds_read_b128 v[38:41], %[bA] offset:64\n\t"
    "ds_read_b64 v[42:43], %[bA] offset:80\n\t"
    "ds_read_b128 a[196:199], %[bX] offset:224\n\t"
    "ds_read_b128 a[132:135], %[bX] offset:480\n\t"
    "ds_read_b128 a[68:71], %[bX] offset:736\n\t"
    "ds_read_b128 a[4:7], %[bX] offset:992\n\t"
    "s_waitcnt lgkmcnt(9)\n\t"
    "v_mfma_f32_16x16x32_bf16 v[0:3], v[32:35], a[192:195], 0\n\t"
    "ds_read_b128 v[44:47], %[bA] offset:128\n\t"
    "v_mfma_f32_16x16x32_bf16 v[4:7], v[34:37], a[192:195], 0\n\t"
    "ds_read_b64 v[48:49], %[bA] offset:144\n\t"
    "s_waitcnt lgkmcnt(10)\n\t"
    "v_mfma_f32_16x16x32_bf16 v[8:11], v[32:35], a[128:131], 0\n\t"
    "ds_read_b128 a[200:203], %[bX] offset:208\n\t"
    "v_mfma_f32_16x16x32_bf16 v[12:15], v[34:37], a[128:131], 0\n\t"
    "ds_read_b128 a[136:139], %[bX] offset:464\n\t"
    "s_waitcnt lgkmcnt(11)\n\t"
    "v_mfma_f32_16x16x32_bf16 v[16:19], v[32:35], a[64:67], 0\n\t"
    "ds_read_b128 a[72:75], %[bX] offset:720\n\t"
    "v_mfma_f32_16x16x32_bf16 v[20:23], v[34:37], a[64:67], 0\n\t"
    "ds_read_b128 a[8:11], %[bX] offset:976\n\t"
    "s_waitcnt lgkmcnt(12)\n\t"
    "v_mfma_f32_16x16x32_bf16 v[24:27], v[32:35], a[0:3], 0\n\t"
    "v_mfma_f32_16x16x32_bf16 v[28:31], v[34:37], a[0:3], 0\n\t"
    "s_cmp_lg_u32 %[wid], 0\n\t"
    "s_cbranch_scc1 L_gp_%=\n\t"
    "global_load_dword v96, %[guo], %[pC] sc1\n\t"
    "L_gp_%=:\n\t"
    "s_waitcnt lgkmcnt(9)\n\t"
    "v_mfma_f32_16x16x32_bf16 v[0:3], v[38:41], a[196:199], v[0:3]\n\t"
    "ds_read_b128 v[50:53], %[bA] offset:192\n\t"
    "v_mfma_f32_16x16x32_bf16 v[4:7], v[40:43], a[196:199], v[4:7]\n\t"
    "ds_read_b64 v[54:55], %[bA] offset:208\n\t"
    "s_waitcnt lgkmcnt(10)\n\t"
    "v_mfma_f32_16x16x32_bf16 v[8:11], v[38:41], a[132:135], v[8:11]\n\t"
    "ds_read_b128 a[204:207], %[bX] offset:192\n\t"
    "v_mfma_f32_16x16x32_bf16 v[12:15], v[40:43], a[132:135], v[12:15]\n\t"
    "ds_read_b128 a[140:143], %[bX] offset:448\n\t"
    "s_waitcnt lgkmcnt(11)\n\t"
    "v_mfma_f32_16x16x32_bf16 v[16:19], v[38:41], a[68:71], v[16:19]\n\t"
    "ds_read_b128 a[76:79], %[bX] offset:704\n\t"
    "v_mfma_f32_16x16x32_bf16 v[20:23], v[40:43], a[68:71], v[20:23]\n\t"
    "ds_read_b128 a[12:15], %[bX] offset:960\n\t"
    "s_waitcnt lgkmcnt(12)\n\t"
    "v_mfma_f32_16x16x32_bf16 v[24:27], v[38:41], a[4:7], v[24:27]\n\t"
    "v_mfma_f32_16x16x32_bf16 v[28:31], v[40:43], a[4:7], v[28:31]\n\t"
    "s_waitcnt lgkmcnt(9)\n\t"
    "v_mfma_f32_16x16x32_bf16 v[0:3], v[44:47], a[200:203], v[0:3]\n\t"
    "ds_read_b128 v[56:59], %[bA] offset:256\n\t"
    "v_mfma_f32_16x16x32_bf16 v[0:3], %[ds0], a[192:195], v[0:3]\n\t"
    "v_mfma_f32_16x16x32_bf16 v[4:7], v[46:49], a[200:203], v[4:7]\n\t"
    "ds_read_b64 v[60:61], %[bA] offset:272\n\t"
    "v_mfma_f32_16x16x32_bf16 v[4:7], %[ds1], a[192:195], v[4:7]\n\t"
    "s_waitcnt lgkmcnt(10)\n\t"
    "v_mfma_f32_16x16x32_bf16 v[8:11], v[44:47], a[136:139], v[8:11]\n\t"
    "ds_read_b128 a[208:211], %[bX] offset:176\n\t"
    "v_mfma_f32_16x16x32_bf16 v[8:11], %[ds0], a[128:131], v[8:11]\n\t"
    "v_mfma_f32_16x16x32_bf16 v[12:15], v[46:49], a[136:139], v[12:15]\n\t"
    "ds_read_b128 a[144:147], %[bX] offset:432\n\t"
    "v_mfma_f32_16x16x32_bf16 v[12:15], %[ds1], a[128:131], v[12:15]\n\t"
    "s_waitcnt lgkmcnt(11)\n\t"
    "v_mfma_f32_16x16x32_bf16 v[16:19], v[44:47], a[72:75], v[16:19]\n\t"
    "ds_read_b128 a[80:83], %[bX] offset:688\n\t"
    "v_mfma_f32_16x16x32_bf16 v[16:19], %[ds0], a[64:67], v[16:19]\n\t"
    "v_mfma_f32_16x16x32_bf16 v[20:23], v[46:49], a[72:75], v[20:23]\n\t"
    "ds_read_b128 a[16:19], %[bX] offset:944\n\t"
    "v_mfma_f32_16x16x32_bf16 v[20:23], %[ds1], a[64:67], v[20:23]\n\t"
    "s_waitcnt lgkmcnt(12)\n\t"
    "v_mfma_f32_16x16x32_bf16 v[24:27], v[44:47], a[8:11], v[24:27]\n\t"
    "v_mfma_f32_16x16x32_bf16 v[24:27], %[ds0], a[0:3], v[24:27]\n\t"
    "v_mfma_f32_16x16x32_bf16 v[28:31], v[46:49], a[8:11], v[28:31]\n\t"
    "v_mfma_f32_16x16x32_bf16 v[28:31], %[ds1], a[0:3], v[28:31]\n\t"
    "s_waitcnt lgkmcnt(9)\n\t"
    "v_mfma_f32_16x16x32_bf16 v[0:3], v[50:53], a[204:207], v[0:3]\n\t"
    "ds_read_b128 v[62:65], %[bA] offset:320\n\t"
    "v_mfma_f32_16x16x32_bf16 v[4:7], v[52:55], a[204:207], v[4:7]\n\t"
    "ds_read_b64 v[66:67], %[bA] offset:336\n\t"
    "s_waitcnt lgkmcnt(10)\n\t"
    "v_mfma_f32_16x16x32_bf16 v[8:11], v[50:53], a[140:143], v[8:11]\n\t"
    "ds_read_b128 a[212:215], %[bX] offset:160\n\t"
    "v_mfma_f32_16x16x32_bf16 v[12:15], v[52:55], a[140:143], v[12:15]\n\t"
    "ds_read_b128 a[148:151], %[bX] offset:416\n\t"
    "s_waitcnt lgkmcnt(11)\n\t"
    "v_mfma_f32_16x16x32_bf16 v[16:19], v[50:53], a[76:79], v[16:19]\n\t"
    "ds_read_b128 a[84:87], %[bX] offset:672\n\t"
    "v_mfma_f32_16x16x32_bf16 v[20:23], v[52:55], a[76:79], v[20:23]\n\t"
    "ds_read_b128 a[20:23], %[bX] offset:928\n\t"
    "s_waitcnt lgkmcnt(12)\n\t"
    "v_mfma_f32_16x16x32_bf16 v[24:27], v[50:53], a[12:15], v[24:27]\n\t"
    "v_mfma_f32_16x16x32_bf16 v[28:31], v[52:55], a[12:15], v[28:31]\n\t"
    "s_waitcnt lgkmcnt(9)\n\t"
    "v_mfma_f32_16x16x32_bf16 v[0:3], v[56:59], a[208:211], v[0:3]\n\t"
    "ds_read_b128 v[68:71], %[bA] offset:384\n\t"
    "v_mfma_f32_16x16x32_bf16 v[4:7], v[58:61], a[208:211], v[4:7]\n\t"
    "ds_read_b64 v[72:73], %[bA] offset:400\n\t"
    "s_waitcnt lgkmcnt(10)\n\t"
    "v_mfma_f32_16x16x32_bf16 v[8:11], v[56:59], a[144:147], v[8:11]\n\t"
    "ds_read_b128 a[216:219], %[bX] offset:144\n\t"
    "v_mfma_f32_16x16x32_bf16 v[12:15], v[58:61], a[144:147], v[12:15]\n\t"
    "ds_read_b128 a[152:155], %[bX] offset:400\n\t"
    "s_waitcnt lgkmcnt(11)\n\t"
    "v_mfma_f32_16x16x32_bf16 v[16:19], v[56:59], a[80:83], v[16:19]\n\t"
    "ds_read_b128 a[88:91], %[bX] offset:656\n\t"
    "v_mfma_f32_16x16x32_bf16 v[20:23], v[58:61], a[80:83], v[20:23]\n\t"
    "ds_read_b128 a[24:27], %[bX] offset:912\n\t"
    "s_waitcnt lgkmcnt(12)\n\t"
    "v_mfma_f32_16x16x32_bf16 v[24:27], v[56:59], a[16:19], v[24:27]\n\t"
    "v_mfma_f32_16x16x32_bf16 v[28:31], v[58:61], a[16:19], v[28:31]\n\t"
    "s_waitcnt lgkmcnt(9)\n\t"
    "v_mfma_f32_16x16x32_bf16 v[0:3], v[62:65], a[212:215], v[0:3]\n\t"
    "ds_read_b128 v[74:77], %[bA] offset:448\n\t"
    "v_mfma_f32_16x16x32_bf16 v[4:7], v[64:67], a[212:215], v[4:7]\n\t"
    "ds_read_b64 v[78:79], %[bA] offset:464\n\t"
    "s_waitcnt lgkmcnt(10)\n\t"
    "v_mfma_f32_16x16x32_bf16 v[8:11], v[62:65], a[148:151], v[8:11]\n\t"
    "ds_read_b128 a[220:223], %[bX] offset:128\n\t"
    "v_mfma_f32_16x16x32_bf16 v[12:15], v[64:67], a[148:151], v[12:15]\n\t"
    "ds_read_b128 a[156:159], %[bX] offset:384\n\t"
    "s_waitcnt lgkmcnt(11)\n\t"
    "v_mfma_f32_16x16x32_bf16 v[16:19], v[62:65], a[84:87], v[16:19]\n\t"
    "ds_read_b128 a[92:95], %[bX] offset:640\n\t"
    "v_mfma_f32_16x16x32_bf16 v[20:23], v[64:67], a[84:87], v[20:23]\n\t"
    "ds_read_b128 a[28:31], %[bX] offset:896\n\t"
    "s_waitcnt lgkmcnt(12)\n\t"
    "v_mfma_f32_16x16x32_bf16 v[24:27], v[62:65], a[20:23], v[24:27]\n\t"
    "v_mfma_f32_16x16x32_bf16 v[28:31], v[64:67], a[20:23], v[28:31]\n\t"
    "s_cmp_lg_u32 %[wid], 0\n\t"
    "s_cbranch_scc1 L_guok_%=\n\t"
    "s_waitcnt vmcnt(0)\n\t"
    "s_mov_b32 s56, 0x20000\n\t"
    "L_gu_%=:\n\t"
    "v_cmp_gt_u32 vcc, 64, v96\n\t"
    "s_sub_u32 s56, s56, 1\n\t"
    "s_cmp_eq_u64 vcc, 0\n\t"
    "s_cbranch_scc1 L_guok_%=\n\t"
    "s_cmp_eq_u32 s56, 0\n\t"
    "s_cbranch_scc1 L_guok_%=\n\t"
    "s_sleep 8\n\t"
    "global_load_dword v96, %[guo], %[pC] sc1\n\t"
    "s_waitcnt vmcnt(0)\n\t"
    "s_branch L_gu_%=\n\t"
    "L_guok_%=:\n\t"
    "s_barrier\n\t"
    "s_waitcnt lgkmcnt(9)\n\t"
    "v_mfma_f32_16x16x32_bf16 v[0:3], v[68:71], a[216:219], v[0:3]\n\t"
    "ds_read_b128 v[32:35], %[bA] offset:512\n\t"
    "v_mfma_f32_16x16x32_bf16 v[4:7], v[70:73], a[216:219], v[4:7]\n\t"
    "ds_read_b64 v[36:37], %[bA] offset:528\n\t"
    "s_waitcnt lgkmcnt(10)\n\t"
    "v_mfma_f32_16x16x32_bf16 v[8:11], v[68:71], a[152:155], v[8:11]\n\t"
    "ds_read_b128 a[224:227], %[bX] offset:112\n\t"
    "v_mfma_f32_16x16x32_bf16 v[12:15], v[70:73], a[152:155], v[12:15]\n\t"
    "ds_read_b128 a[160:163], %[bX] offset:368\n\t"
    "s_waitcnt lgkmcnt(11)\n\t"
    "v_mfma_f32_16x16x32_bf16 v[16:19], v[68:71], a[88:91], v[16:19]\n\t"
    "ds_read_b128 a[96:99], %[bX] offset:624\n\t"
    "v_mfma_f32_16x16x32_bf16 v[20:23], v[70:73], a[88:91], v[20:23]\n\t"
    "ds_read_b128 a[32:35], %[bX] offset:880\n\t"
    "s_waitcnt lgkmcnt(12)\n\t"
    "v_mfma_f32_16x16x32_bf16 v[24:27], v[68:71], a[24:27], v[24:27]\n\t"
    "s_add_u32 s52, %[us], 0\n\t"
    "buffer_load_dwordx4 v[120:123], %[vx], s[48:51], s52 offen sc1\n\t"
    "v_mfma_f32_16x16x32_bf16 v[28:31], v[70:73], a[24:27], v[28:31]\n\t"
    "s_waitcnt lgkmcnt(9)\n\t"
    "v_mfma_f32_16x16x32_bf16 v[0:3], v[74:77], a[220:223], v[0:3]\n\t"
    "ds_read_b128 v[38:41], %[bA] offset:576\n\t"
    "v_mfma_f32_16x16x32_bf16 v[4:7], v[76:79], a[220:223], v[4:7]\n\t"
    "ds_read_b64 v[42:43], %[bA] offset:592\n\t"
    "s_waitcnt lgkmcnt(10)\n\t"
    "v_mfma_f32_16x16x32_bf16 v[8:11], v[74:77], a[156:159], v[8:11]\n\t"
    "ds_read_b128 a[228:231], %[bX] offset:96\n\t"
    "v_mfma_f32_16x16x32_bf16 v[12:15], v[76:79], a[156:159], v[12:15]\n\t"
    "ds_read_b128 a[164:167], %[bX] offset:352\n\t"
    "s_waitcnt lgkmcnt(11)\n\t"
    "v_mfma_f32_16x16x32_bf16 v[16:19], v[74:77], a[92:95], v[16:19]\n\t"
    "ds_read_b128 a[100:103], %[bX] offset:608\n\t"
    "v_mfma_f32_16x16x32_bf16 v[20:23], v[76:79], a[92:95], v[20:23]\n\t"
    "ds_read_b128 a[36:39], %[bX] offset:864\n\t"
    "s_waitcnt lgkmcnt(12)\n\t"
    "v_mfma_f32_16x16x32_bf16 v[24:27], v[74:77], a[28:31], v[24:27]\n\t"
    "s_add_u32 s52, %[us], 148480\n\t"
    "buffer_load_dwordx4 v[124:127], %[vx], s[48:51], s52 offen sc1\n\t"
    "v_mfma_f32_16x16x32_bf16 v[28:31], v[76:79], a[28:31], v[28:31]\n\t"
    "s_waitcnt lgkmcnt(9)\n\t"
    "v_mfma_f32_16x16x32_bf16 v[0:3], v[32:35], a[224:227], v[0:3]\n\t"
    "ds_read_b128 v[44:47], %[bA] offset:640\n\t"
    "v_mfma_f32_16x16x32_bf16 v[4:7], v[34:37], a[224:227], v[4:7]\n\t"
    "ds_read_b64 v[48:49], %[bA] offset:656\n\t"
    "s_waitcnt lgkmcnt(10)\n\t"
    "v_mfma_f32_16x16x32_bf16 v[8:11], v[32:35], a[160:163], v[8:11]\n\t"
    "ds_read_b128 a[232:235], %[bX] offset:80\n\t"
    "v_mfma_f32_16x16x32_bf16 v[12:15], v[34:37], a[160:163], v[12:15]\n\t"
    "ds_read_b128 a[168:171], %[bX] offset:336\n\t"
    "s_waitcnt lgkmcnt(11)\n\t"
    "v_mfma_f32_16x16x32_bf16 v[16:19], v[32:35], a[96:99], v[16:19]\n\t"
    "ds_read_b128 a[104:107], %[bX] offset:592\n\t"
    "v_mfma_f32_16x16x32_bf16 v[20:23], v[34:37], a[96:99], v[20:23]\n\t"
    "ds_read_b128 a[40:43], %[bX] offset:848\n\t"
    "s_waitcnt lgkmcnt(12)\n\t"
    "v_mfma_f32_16x16x32_bf16 v[24:27], v[32:35], a[32:35], v[24:27]\n\t"
    "s_add_u32 s52, %[us], 296960\n\t"
    "buffer_load_dwordx4 v[128:131], %[vx], s[48:51], s52 offen sc1\n\t"
    "v_mfma_f32_16x16x32_bf16 v[28:31], v[34:37], a[32:35], v[28:31]\n\t"
    "s_waitcnt lgkmcnt(9)\n\t"
    "v_mfma_f32_16x16x32_bf16 v[0:3], v[38:41], a[228:231], v[0:3]\n\t"
    "ds_read_b128 v[50:53], %[bA] offset:704\n\t"
    "v_mfma_f32_16x16x32_bf16 v[4:7], v[40:43], a[228:231], v[4:7]\n\t"
    "ds_read_b64 v[54:55], %[bA] offset:720\n\t"
    "s_waitcnt lgkmcnt(10)\n\t"
    "v_mfma_f32_16x16x32_bf16 v[8:11], v[38:41], a[164:167], v[8:11]\n\t"
    "ds_read_b128 a[236:239], %[bX] offset:64\n\t"
    "v_mfma_f32_16x16x32_bf16 v[12:15], v[40:43], a[164:167], v[12:15]\n\t"
    "ds_read_b128 a[172:175], %[bX] offset:320\n\t"
    "s_waitcnt lgkmcnt(11)\n\t"
    "v_mfma_f32_16x16x32_bf16 v[16:19], v[38:41], a[100:103], v[16:19]\n\t"
    "ds_read_b128 a[108:111], %[bX] offset:576\n\t"
    "v_mfma_f32_16x16x32_bf16 v[20:23], v[40:43], a[100:103], v[20:23]\n\t"
    "ds_read_b128 a[44:47], %[bX] offset:832\n\t"
    "s_waitcnt lgkmcnt(12)\n\t"
    "v_mfma_f32_16x16x32_bf16 v[24:27], v[38:41], a[36:39], v[24:27]\n\t"
    "s_add_u32 s52, %[us], 445440\n\t"
    "buffer_load_dwordx4 v[132:135], %[vx], s[48:51], s52 offen sc1\n\t"
    "v_mfma_f32_16x16x32_bf16 v[28:31], v[40:43], a[36:39], v[28:31]\n\t"
    "s_waitcnt lgkmcnt(9)\n\t"
    "v_mfma_f32_16x16x32_bf16 v[0:3], v[44:47], a[232:235], v[0:3]\n\t"
    "ds_read_b128 v[56:59], %[bA] offset:768\n\t"
    "v_mfma_f32_16x16x32_bf16 v[4:7], v[46:49], a[232:235], v[4:7]\n\t"
    "ds_read_b64 v[60:61], %[bA] offset:784\n\t"
    "s_waitcnt lgkmcnt(10)\n\t"
    "v_mfma_f32_16x16x32_bf16 v[8:11], v[44:47], a[168:171], v[8:11]\n\t"
    "ds_read_b128 a[240:243], %[bX] offset:48\n\t"
    "v_mfma_f32_16x16x32_bf16 v[12:15], v[46:49], a[168:171], v[12:15]\n\t"
    "ds_read_b128 a[176:179], %[bX] offset:304\n\t"
    "s_waitcnt lgkmcnt(11)\n\t"
    "v_mfma_f32_16x16x32_bf16 v[16:19], v[44:47], a[104:107], v[16:19]\n\t"
    "ds_read_b128 a[112:115], %[bX] offset:560\n\t"
    "v_mfma_f32_16x16x32_bf16 v[20:23], v[46:49], a[104:107], v[20:23]\n\t"
    "ds_read_b128 a[48:51], %[bX] offset:816\n\t"
    "s_waitcnt lgkmcnt(12)\n\t"
    "v_mfma_f32_16x16x32_bf16 v[24:27], v[44:47], a[40:43], v[24:27]\n\t"
    "buffer_load_dwordx4 v[136:139], %[sv], s[48:51], 0 offen offset:0 sc1\n\t"
    "v_mfma_f32_16x16x32_bf16 v[28:31], v[46:49], a[40:43], v[28:31]\n\t"
    "s_waitcnt lgkmcnt(9)\n\t"
    "v_mfma_f32_16x16x32_bf16 v[0:3], v[50:53], a[236:239], v[0:3]\n\t"
    "ds_read_b128 v[62:65], %[bA] offset:832\n\t"
    "v_mfma_f32_16x16x32_bf16 v[4:7], v[52:55], a[236:239], v[4:7]\n\t"
    "ds_read_b64 v[66:67], %[bA] offset:848\n\t"
    "s_waitcnt lgkmcnt(10)\n\t"
    "v_mfma_f32_16x16x32_bf16 v[8:11], v[50:53], a[172:175], v[8:11]\n\t"
    "ds_read_b128 a[244:247], %[bX] offset:32\n\t"
    "v_mfma_f32_16x16x32_bf16 v[12:15], v[52:55], a[172:175], v[12:15]\n\t"
    "ds_read_b128 a[180:183], %[bX] offset:288\n\t"
    "s_waitcnt lgkmcnt(11)\n\t"
    "v_mfma_f32_16x16x32_bf16 v[16:19], v[50:53], a[108:111], v[16:19]\n\t"
    "ds_read_b128 a[116:119], %[bX] offset:544\n\t"
    "v_mfma_f32_16x16x32_bf16 v[20:23], v[52:55], a[108:111], v[20:23]\n\t"
    "ds_read_b128 a[52:55], %[bX] offset:800\n\t"
    "s_waitcnt lgkmcnt(12)\n\t"
    "v_mfma_f32_16x16x32_bf16 v[24:27], v[50:53], a[44:47], v[24:27]\n\t"
    "buffer_load_dwordx4 v[140:143], %[sv], s[48:51], 0 offen offset:1024 sc1\n\t"
    "v_mfma_f32_16x16x32_bf16 v[28:31], v[52:55], a[44:47], v[28:31]\n\t"
    "s_waitcnt lgkmcnt(9)\n\t"
    "v_mfma_f32_16x16x32_bf16 v[0:3], v[56:59], a[240:243], v[0:3]\n\t"
    "ds_read_b128 v[68:71], %[bA] offset:896\n\t"
    "v_mfma_f32_16x16x32_bf16 v[4:7], v[58:61], a[240:243], v[4:7]\n\t"
    "ds_read_b64 v[72:73], %[bA] offset:912\n\t"
    "s_waitcnt lgkmcnt(10)\n\t"
    "v_mfma_f32_16x16x32_bf16 v[8:11], v[56:59], a[176:179], v[8:11]\n\t"
    "ds_read_b128 a[248:251], %[bX] offset:16\n\t"
    "v_mfma_f32_16x16x32_bf16 v[12:15], v[58:61], a[176:179], v[12:15]\n\t"
    "ds_read_b128 a[184:187], %[bX] offset:272\n\t"
    "s_waitcnt lgkmcnt(11)\n\t"
    "v_mfma_f32_16x16x32_bf16 v[16:19], v[56:59], a[112:115], v[16:19]\n\t"
    "ds_read_b128 a[120:123], %[bX] offset:528\n\t"
    "v_mfma_f32_16x16x32_bf16 v[20:23], v[58:61], a[112:115], v[20:23]\n\t"
    "ds_read_b128 a[56:59], %[bX] offset:784\n\t"
    "s_waitcnt lgkmcnt(12)\n\t"
    "v_mfma_f32_16x16x32_bf16 v[24:27], v[56:59], a[48:51], v[24:27]\n\t"
    "buffer_load_dwordx4 v[144:147], %[sv], s[48:51], 0 offen offset:2048 sc1\n\t"
    "v_mfma_f32_16x16x32_bf16 v[28:31], v[58:61], a[48:51], v[28:31]\n\t"
    "s_waitcnt lgkmcnt(9)\n\t"
    "v_mfma_f32_16x16x32_bf16 v[0:3], v[62:65], a[244:247], v[0:3]\n\t"
    "ds_read_b128 v[74:77], %[bA] offset:960\n\t"
    "v_mfma_f32_16x16x32_bf16 v[4:7], v[64:67], a[244:247], v[4:7]\n\t"
    "ds_read_b64 v[78:79], %[bA] offset:976\n\t"
    "s_waitcnt lgkmcnt(10)\n\t"
    "v_mfma_f32_16x16x32_bf16 v[8:11], v[62:65], a[180:183], v[8:11]\n\t"
    "ds_read_b128 a[252:255], %[bX] offset:0\n\t"
    "v_mfma_f32_16x16x32_bf16 v[12:15], v[64:67], a[180:183], v[12:15]\n\t"
    "ds_read_b128 a[188:191], %[bX] offset:256\n\t"
    "s_waitcnt lgkmcnt(11)\n\t"
    "v_mfma_f32_16x16x32_bf16 v[16:19], v[62:65], a[116:119], v[16:19]\n\t"
    "ds_read_b128 a[124:127], %[bX] offset:512\n\t"
    "v_mfma_f32_16x16x32_bf16 v[20:23], v[64:67], a[116:119], v[20:23]\n\t"
    "ds_read_b128 a[60:63], %[bX] offset:768\n\t"
    "s_waitcnt lgkmcnt(12)\n\t"
    "v_mfma_f32_16x16x32_bf16 v[24:27], v[62:65], a[52:55], v[24:27]\n\t"
    "buffer_load_dwordx4 v[148:151], %[sv], s[48:51], 0 offen offset:3072 sc1\n\t"
    "v_mfma_f32_16x16x32_bf16 v[28:31], v[64:67], a[52:55], v[28:31]\n\t"
    "s_waitcnt lgkmcnt(9)\n\t"
    "v_mfma_f32_16x16x32_bf16 v[0:3], v[68:71], a[248:251], v[0:3]\n\t"
    "ds_read_b128 v[32:35], %[bA] offset:1024\n\t"
    "v_mfma_f32_16x16x32_bf16 v[4:7], v[70:73], a[248:251], v[4:7]\n\t"
    "ds_read_b64 v[36:37], %[bA] offset:1040\n\t"
    "s_waitcnt lgkmcnt(10)\n\t"
    "v_mfma_f32_16x16x32_bf16 v[8:11], v[68:71], a[184:187], v[8:11]\n\t"
    "ds_read_b128 v[38:41], %[bA] offset:1088\n\t"
    "v_mfma_f32_16x16x32_bf16 v[12:15], v[70:73], a[184:187], v[12:15]\n\t"
    "ds_read_b64 v[42:43], %[bA] offset:1104\n\t"
    "s_waitcnt lgkmcnt(11)\n\t"
    "v_mfma_f32_16x16x32_bf16 v[16:19], v[68:71], a[120:123], v[16:19]\n\t"
    "v_mfma_f32_16x16x32_bf16 v[20:23], v[70:73], a[120:123], v[20:23]\n\t"
    "s_waitcnt lgkmcnt(10)\n\t"
    "v_mfma_f32_16x16x32_bf16 v[24:27], v[68:71], a[56:59], v[24:27]\n\t"
    "v_mfma_f32_16x16x32_bf16 v[28:31], v[70:73], a[56:59], v[28:31]\n\t"
    "s_waitcnt lgkmcnt(7)\n\t"
    "v_mfma_f32_16x16x32_bf16 v[0:3], v[74:77], a[252:255], v[0:3]\n\t"
    "ds_read_b128 v[44:47], %[bA] offset:1152\n\t"
    "v_mfma_f32_16x16x32_bf16 v[4:7], v[76:79], a[252:255], v[4:7]\n\t"
    "ds_read_b64 v[48:49], %[bA] offset:1168\n\t"
    "s_waitcnt lgkmcnt(8)\n\t"
    "v_mfma_f32_16x16x32_bf16 v[8:11], v[74:77], a[188:191], v[8:11]\n\t"
    "v_mfma_f32_16x16x32_bf16 v[12:15], v[76:79], a[188:191], v[12:15]\n\t"
    "s_waitcnt lgkmcnt(7)\n\t"
    "v_mfma_f32_16x16x32_bf16 v[16:19], v[74:77], a[124:127], v[16:19]\n\t"
    "v_mfma_f32_16x16x32_bf16 v[20:23], v[76:79], a[124:127], v[20:23]\n\t"
    "s_waitcnt lgkmcnt(6)\n\t"
    "v_mfma_f32_16x16x32_bf16 v[24:27], v[74:77], a[60:63], v[24:27]\n\t"
    "v_mfma_f32_16x16x32_bf16 v[28:31], v[76:79], a[60:63], v[28:31]\n\t"
    "s_waitcnt lgkmcnt(5)\n\t"
    "v_mfma_f32_16x16x32_bf16 v[8:11], v[32:35], a[192:195], v[8:11]\n\t"
    "ds_read_b128 v[50:53], %[bA] offset:1216\n\t"
    "s_waitcnt lgkmcnt(5)\n\t"
    "v_mfma_f32_16x16x32_bf16 v[12:15], v[34:37], a[192:195], v[12:15]\n\t"
    "ds_read_b64 v[54:55], %[bA] offset:1232\n\t"
    "v_mfma_f32_16x16x32_bf16 v[16:19], v[32:35], a[128:131], v[16:19]\n\t"
    "ds_read_b128 v[56:59], %[bA] offset:1280\n\t"
    "v_mfma_f32_16x16x32_bf16 v[20:23], v[34:37], a[128:131], v[20:23]\n\t"
    "ds_read_b64 v[60:61], %[bA] offset:1296\n\t"
    "v_mfma_f32_16x16x32_bf16 v[24:27], v[32:35], a[64:67], v[24:27]\n\t"
    "v_mfma_f32_16x16x32_bf16 v[28:31], v[34:37], a[64:67], v[28:31]\n\t"
    "s_waitcnt lgkmcnt(7)\n\t"
    "v_mfma_f32_16x16x32_bf16 v[8:11], v[38:41], a[196:199], v[8:11]\n\t"
    "ds_read_b128 v[62:65], %[bA] offset:1344\n\t"
    "s_waitcnt lgkmcnt(7)\n\t"
    "v_mfma_f32_16x16x32_bf16 v[12:15], v[40:43], a[196:199], v[12:15]\n\t"
    "ds_read_b64 v[66:67], %[bA] offset:1360\n\t"
    "v_mfma_f32_16x16x32_bf16 v[16:19], v[38:41], a[132:135], v[16:19]\n\t"
    "v_mfma_f32_16x16x32_bf16 v[20:23], v[40:43], a[132:135], v[20:23]\n\t"
    "v_mfma_f32_16x16x32_bf16 v[24:27], v[38:41], a[68:71], v[24:27]\n\t"
    "v_mfma_f32_16x16x32_bf16 v[28:31], v[40:43], a[68:71], v[28:31]\n\t"
    "s_waitcnt lgkmcnt(7)\n\t"
    "v_mfma_f32_16x16x32_bf16 v[8:11], v[44:47], a[200:203], v[8:11]\n\t"
    "ds_read_b128 v[68:71], %[bA] offset:1408\n\t"
    "s_waitcnt lgkmcnt(7)\n\t"
    "v_mfma_f32_16x16x32_bf16 v[12:15], v[46:49], a[200:203], v[12:15]\n\t"
    "ds_read_b64 v[72:73], %[bA] offset:1424\n\t"
    "v_mfma_f32_16x16x32_bf16 v[16:19], v[44:47], a[136:139], v[16:19]\n\t"
    "v_mfma_f32_16x16x32_bf16 v[20:23], v[46:49], a[136:139], v[20:23]\n\t"
    "v_mfma_f32_16x16x32_bf16 v[24:27], v[44:47], a[72:75], v[24:27]\n\t"
    "v_mfma_f32_16x16x32_bf16 v[28:31], v[46:49], a[72:75], v[28:31]\n\t"
    "s_waitcnt lgkmcnt(7)\n\t"
    "v_mfma_f32_16x16x32_bf16 v[8:11], v[50:53], a[204:207], v[8:11]\n\t"
    "ds_read_b128 v[74:77], %[bA] offset:1472\n\t"
    "s_waitcnt lgkmcnt(7)\n\t"
    "v_mfma_f32_16x16x32_bf16 v[12:15], v[52:55], a[204:207], v[12:15]\n\t"
    "ds_read_b64 v[78:79], %[bA] offset:1488\n\t"
    "v_mfma_f32_16x16x32_bf16 v[16:19], v[50:53], a[140:143], v[16:19]\n\t"
    "v_mfma_f32_16x16x32_bf16 v[20:23], v[52:55], a[140:143], v[20:23]\n\t"
    "v_mfma_f32_16x16x32_bf16 v[24:27], v[50:53], a[76:79], v[24:27]\n\t"
    "v_mfma_f32_16x16x32_bf16 v[28:31], v[52:55], a[76:79], v[28:31]\n\t"
    "s_waitcnt lgkmcnt(7)\n\t"
    "v_mfma_f32_16x16x32_bf16 v[8:11], v[56:59], a[208:211], v[8:11]\n\t"
    "ds_read_b128 v[32:35], %[bA] offset:1536\n\t"
    "s_waitcnt vmcnt(7)\n\t"
    "ds_write_b128 %[uw], v[120:123] offset:0\n\t"
    "s_waitcnt lgkmcnt(8)\n\t"
    "v_mfma_f32_16x16x32_bf16 v[12:15], v[58:61], a[208:211], v[12:15]\n\t"
    "ds_read_b64 v[36:37], %[bA] offset:1552\n\t"
    "v_mfma_f32_16x16x32_bf16 v[16:19], v[56:59], a[144:147], v[16:19]\n\t"
    "s_waitcnt vmcnt(6)\n\t"
    "ds_write_b128 %[uw], v[124:127] offset:64\n\t"
    "v_mfma_f32_16x16x32_bf16 v[20:23], v[58:61], a[144:147], v[20:23]\n\t"
    "v_mfma_f32_16x16x32_bf16 v[24:27], v[56:59], a[80:83], v[24:27]\n\t"
    "s_waitcnt vmcnt(5)\n\t"
    "ds_write_b128 %[uw], v[128:131] offset:128\n\t"
    "v_mfma_f32_16x16x32_bf16 v[28:31], v[58:61], a[80:83], v[28:31]\n\t"
    "s_waitcnt lgkmcnt(10)\n\t"
    "v_mfma_f32_16x16x32_bf16 v[8:11], v[62:65], a[212:215], v[8:11]\n\t"
    "ds_read_b128 v[38:41], %[bA] offset:1600\n\t"
    "s_waitcnt vmcnt(4)\n\t"
    "ds_write_b128 %[uw], v[132:135] offset:192\n\t"
    "s_waitcnt lgkmcnt(11)\n\t"
    "v_mfma_f32_16x16x32_bf16 v[12:15], v[64:67], a[212:215], v[12:15]\n\t"
    "ds_read_b64 v[42:43], %[bA] offset:1616\n\t"
    "v_mfma_f32_16x16x32_bf16 v[16:19], v[62:65], a[148:151], v[16:19]\n\t"
    "s_waitcnt vmcnt(3)\n\t"
    "ds_write_b128 %[sw], v[136:139] offset:0\n\t"
    "v_mfma_f32_16x16x32_bf16 v[20:23], v[64:67], a[148:151], v[20:23]\n\t"
    "v_mfma_f32_16x16x32_bf16 v[24:27], v[62:65], a[84:87], v[24:27]\n\t"
    "s_waitcnt vmcnt(2)\n\t"
    "ds_write_b128 %[sw], v[140:143] offset:64\n\t"
    "v_mfma_f32_16x16x32_bf16 v[28:31], v[64:67], a[84:87], v[28:31]\n\t"
    "s_waitcnt lgkmcnt(13)\n\t"
    "v_mfma_f32_16x16x32_bf16 v[8:11], v[68:71], a[216:219], v[8:11]\n\t"
    "ds_read_b128 v[44:47], %[bA] offset:1664\n\t"
    "s_waitcnt vmcnt(1)\n\t"
    "ds_write_b128 %[sw], v[144:147] offset:128\n\t"
    "s_waitcnt lgkmcnt(14)\n\t"
    "v_mfma_f32_16x16x32_bf16 v[12:15], v[70:73], a[216:219], v[12:15]\n\t"
    "ds_read_b64 v[48:49], %[bA] offset:1680\n\t"
    "v_mfma_f32_16x16x32_bf16 v[16:19], v[68:71], a[152:155], v[16:19]\n\t"
    "s_waitcnt vmcnt(0)\n\t"
    "s_waitcnt lgkmcnt(14)\n\t"
    "ds_write_b128 %[sw], v[148:151] offset:192\n\t"
    "v_mfma_f32_16x16x32_bf16 v[20:23], v[70:73], a[152:155], v[20:23]\n\t"
    "v_mfma_f32_16x16x32_bf16 v[24:27], v[68:71], a[88:91], v[24:27]\n\t"
    "v_mfma_f32_16x16x32_bf16 v[28:31], v[70:73], a[88:91], v[28:31]\n\t"
    "s_waitcnt lgkmcnt(0)\n\t"
    "s_barrier\n\t"
    "ds_read_b128 v[80:83], %[uU] offset:0\n\t"
    "ds_read_b128 v[120:123], %[sU] offset:0\n\t"
    "v_mfma_f32_16x16x32_bf16 v[8:11], v[74:77], a[220:223], v[8:11]\n\t"
    "ds_read_b128 v[50:53], %[bA] offset:1728\n\t"
    "ds_read_b128 v[124:127], %[sU] offset:1056\n\t"
    "ds_read_b128 v[128:131], %[sU] offset:2112\n\t"
    "v_mfma_f32_16x16x32_bf16 v[12:15], v[76:79], a[220:223], v[12:15]\n\t"
    "ds_read_b64 v[54:55], %[bA] offset:1744\n\t"
    "ds_read_b128 v[132:135], %[sU] offset:3168\n\t"
    "v_mfma_f32_16x16x32_bf16 v[16:19], v[74:77], a[156:159], v[16:19]\n\t"
    "v_mfma_f32_16x16x32_bf16 v[20:23], v[76:79], a[156:159], v[20:23]\n\t"
    "v_mfma_f32_16x16x32_bf16 v[24:27], v[74:77], a[92:95], v[24:27]\n\t"
    "s_waitcnt lgkmcnt(0)\n\t"
    "v_mfma_f32_16x16x32_bf16 v[28:31], v[76:79], a[92:95], v[28:31]\n\t"
    "v_lshlrev_b32 v104, 16, v80\n\t"
    "v_and_b32 v105, 0xffff0000, v80\n\t"
    "v_mfma_f32_16x16x32_bf16 v[8:11], v[32:35], a[224:227], v[8:11]\n\t"
    "ds_read_b128 v[56:59], %[bA] offset:1792\n\t"
    "v_lshlrev_b32 v106, 16, v81\n\t"
    "v_and_b32 v107, 0xffff0000, v81\n\t"
    "v_mfma_f32_16x16x32_bf16 v[12:15], v[34:37], a[224:227], v[12:15]\n\t"
    "ds_read_b64 v[60:61], %[bA] offset:1808\n\t"
    "v_lshlrev_b32 v108, 16, v82\n\t"
    "v_and_b32 v109, 0xffff0000, v82\n\t"
    "v_mfma_f32_16x16x32_bf16 v[16:19], v[32:35], a[160:163], v[16:19]\n\t"
    "v_lshlrev_b32 v110, 16, v83\n\t"
    "v_and_b32 v111, 0xffff0000, v83\n\t"
    "v_mfma_f32_16x16x32_bf16 v[20:23], v[34:37], a[160:163], v[20:23]\n\t"
    "v_fma_f32 v112, v104, v120, v128\n\t"
    "v_fma_f32 v113, v105, v121, v129\n\t"
    "v_mfma_f32_16x16x32_bf16 v[24:27], v[32:35], a[96:99], v[24:27]\n\t"
    "v_fma_f32 v114, v106, v122, v130\n\t"
    "v_fma_f32 v115, v107, v123, v131\n\t"
    "v_mfma_f32_16x16x32_bf16 v[28:31], v[34:37], a[96:99], v[28:31]\n\t"
    "v_fma_f32 v116, v108, v124, v132\n\t"
    "v_fma_f32 v117, v109, v125, v133\n\t"
    "v_mfma_f32_16x16x32_bf16 v[8:11], v[38:41], a[228:231], v[8:11]\n\t"
    "ds_read_b128 v[62:65], %[bA] offset:1856\n\t"
    "v_fma_f32 v118, v110, v126, v134\n\t"
    "v_fma_f32 v119, v111, v127, v135\n\t"
    "v_mfma_f32_16x16x32_bf16 v[12:15], v[40:43], a[228:231], v[12:15]\n\t"
    "ds_read_b64 v[66:67], %[bA] offset:1872\n\t"
    "v_fmac_f32 v104, v112, v0\n\t"
    "v_fmac_f32 v105, v113, v1\n\t"
    "v_mfma_f32_16x16x32_bf16 v[16:19], v[38:41], a[164:167], v[16:19]\n\t"
    "v_cvt_pk_bf16_f32 v100, v104, v105\n\t"
    "v_fmac_f32 v106, v114, v2\n\t"
    "v_mfma_f32_16x16x32_bf16 v[20:23], v[40:43], a[164:167], v[20:23]\n\t"
    "v_fmac_f32 v107, v115, v3\n\t"
    "v_cvt_pk_bf16_f32 v101, v106, v107\n\t"
    "v_mfma_f32_16x16x32_bf16 v[24:27], v[38:41], a[100:103], v[24:27]\n\t"
    "v_fmac_f32 v108, v116, v4\n\t"
    "v_fmac_f32 v109, v117, v5\n\t"
    "v_mfma_f32_16x16x32_bf16 v[28:31], v[40:43], a[100:103], v[28:31]\n\t"
    "v_cvt_pk_bf16_f32 v102, v108, v109\n\t"
    "v_fmac_f32 v110, v118, v6\n\t"
    "v_mfma_f32_16x16x32_bf16 v[8:11], v[44:47], a[232:235], v[8:11]\n\t"
    "ds_read_b128 v[68:71], %[bA] offset:1920\n\t"
    "v_fmac_f32 v111, v119, v7\n\t"
    "v_cvt_pk_bf16_f32 v103, v110, v111\n\t"
    "v_mfma_f32_16x16x32_bf16 v[12:15], v[46:49], a[232:235], v[12:15]\n\t"
    "ds_read_b64 v[72:73], %[bA] offset:1936\n\t"
    "buffer_store_dwordx4 v[100:103], %[vo], s[44:47], s53 offen sc1\n\t"
    "s_add_u32 s53, s53, 589824\n\t"
    "v_mfma_f32_16x16x32_bf16 v[16:19], v[44:47], a[168:171], v[16:19]\n\t"
    "ds_read_b128 v[80:83], %[uU] offset:5120\n\t"
    "ds_read_b128 v[120:123], %[sU] offset:4352\n\t"
    "v_mfma_f32_16x16x32_bf16 v[20:23], v[46:49], a[168:171], v[20:23]\n\t"
    "ds_read_b128 v[124:127], %[sU] offset:5408\n\t"
    "ds_read_b128 v[128:131], %[sU] offset:6464\n\t"
    "v_mfma_f32_16x16x32_bf16 v[24:27], v[44:47], a[104:107], v[24:27]\n\t"
    "ds_read_b128 v[132:135], %[sU] offset:7520\n\t"
    "v_mfma_f32_16x16x32_bf16 v[28:31], v[46:49], a[104:107], v[28:31]\n\t"
    "v_mfma_f32_16x16x32_bf16 v[8:11], v[50:53], a[236:239], v[8:11]\n\t"
    "ds_read_b128 v[74:77], %[bA] offset:1984\n\t"
    "s_waitcnt lgkmcnt(1)\n\t"
    "v_mfma_f32_16x16x32_bf16 v[12:15], v[52:55], a[236:239], v[12:15]\n\t"
    "ds_read_b64 v[78:79], %[bA] offset:2000\n\t"
    "v_lshlrev_b32 v104, 16, v80\n\t"
    "v_and_b32 v105, 0xffff0000, v80\n\t"
    "v_mfma_f32_16x16x32_bf16 v[16:19], v[50:53], a[172:175], v[16:19]\n\t"
    "v_lshlrev_b32 v106, 16, v81\n\t"
    "v_and_b32 v107, 0xffff0000, v81\n\t"
    "v_mfma_f32_16x16x32_bf16 v[20:23], v[52:55], a[172:175], v[20:23]\n\t"
    "v_lshlrev_b32 v108, 16, v82\n\t"
    "v_and_b32 v109, 0xffff0000, v82\n\t"
    "v_mfma_f32_16x16x32_bf16 v[24:27], v[50:53], a[108:111], v[24:27]\n\t"
    "v_lshlrev_b32 v110, 16, v83\n\t"
    "v_and_b32 v111, 0xffff0000, v83\n\t"
    "v_mfma_f32_16x16x32_bf16 v[28:31], v[52:55], a[108:111], v[28:31]\n\t"
    "v_fma_f32 v112, v104, v120, v128\n\t"
    "v_fma_f32 v113, v105, v121, v129\n\t"
    "v_mfma_f32_16x16x32_bf16 v[8:11], v[56:59], a[240:243], v[8:11]\n\t"
    "ds_read_b128 v[32:35], %[bA] offset:2048\n\t"
    "v_fma_f32 v114, v106, v122, v130\n\t"
    "v_fma_f32 v115, v107, v123, v131\n\t"
    "v_mfma_f32_16x16x32_bf16 v[12:15], v[58:61], a[240:243], v[12:15]\n\t"
    "ds_read_b64 v[36:37], %[bA] offset:2064\n\t"
    "v_fma_f32 v116, v108, v124, v132\n\t"
    "v_fma_f32 v117, v109, v125, v133\n\t"
    "v_mfma_f32_16x16x32_bf16 v[16:19], v[56:59], a[176:179], v[16:19]\n\t"
    "v_fma_f32 v118, v110, v126, v134\n\t"
    "v_fma_f32 v119, v111, v127, v135\n\t"
    "v_mfma_f32_16x16x32_bf16 v[20:23], v[58:61], a[176:179], v[20:23]\n\t"
    "v_mfma_f32_16x16x32_bf16 v[24:27], v[56:59], a[112:115], v[24:27]\n\t"
    "v_mfma_f32_16x16x32_bf16 v[28:31], v[58:61], a[112:115], v[28:31]\n\t"
    "v_mfma_f32_16x16x32_bf16 v[8:11], v[62:65], a[244:247], v[8:11]\n\t"
    "ds_read_b128 v[38:41], %[bA] offset:2112\n\t"
    "v_mfma_f32_16x16x32_bf16 v[12:15], v[64:67], a[244:247], v[12:15]\n\t"
    "ds_read_b64 v[42:43], %[bA] offset:2128\n\t"
    "v_mfma_f32_16x16x32_bf16 v[16:19], v[62:65], a[180:183], v[16:19]\n\t"
    "v_mfma_f32_16x16x32_bf16 v[20:23], v[64:67], a[180:183], v[20:23]\n\t"
    "v_mfma_f32_16x16x32_bf16 v[24:27], v[62:65], a[116:119], v[24:27]\n\t"
    "v_mfma_f32_16x16x32_bf16 v[28:31], v[64:67], a[116:119], v[28:31]\n\t"
    "v_mfma_f32_16x16x32_bf16 v[8:11], v[68:71], a[248:251], v[8:11]\n\t"
    "ds_read_b128 v[44:47], %[bA] offset:2176\n\t"
    "v_mfma_f32_16x16x32_bf16 v[12:15], v[70:73], a[248:251], v[12:15]\n\t"
    "ds_read_b64 v[48:49], %[bA] offset:2192\n\t"
    "v_mfma_f32_16x16x32_bf16 v[16:19], v[68:71], a[184:187], v[16:19]\n\t"
    "v_mfma_f32_16x16x32_bf16 v[20:23], v[70:73], a[184:187], v[20:23]\n\t"
    "v_mfma_f32_16x16x32_bf16 v[24:27], v[68:71], a[120:123], v[24:27]\n\t"
    "v_mfma_f32_16x16x32_bf16 v[28:31], v[70:73], a[120:123], v[28:31]\n\t"
    "s_waitcnt lgkmcnt(7)\n\t"
    "v_mfma_f32_16x16x32_bf16 v[8:11], v[74:77], a[252:255], v[8:11]\n\t"
    "ds_read_b128 v[50:53], %[bA] offset:2240\n\t"
    "s_waitcnt lgkmcnt(7)\n\t"
    "v_mfma_f32_16x16x32_bf16 v[12:15], v[76:79], a[252:255], v[12:15]\n\t"
    "ds_read_b64 v[54:55], %[bA] offset:2256\n\t"
    "v_mfma_f32_16x16x32_bf16 v[16:19], v[74:77], a[188:191], v[16:19]\n\t"
    "v_mfma_f32_16x16x32_bf16 v[20:23], v[76:79], a[188:191], v[20:23]\n\t"
    "v_mfma_f32_16x16x32_bf16 v[24:27], v[74:77], a[124:127], v[24:27]\n\t"
    "v_mfma_f32_16x16x32_bf16 v[28:31], v[76:79], a[124:127], v[28:31]\n\t"
    "s_waitcnt lgkmcnt(7)\n\t"
    "v_mfma_f32_16x16x32_bf16 v[16:19], v[32:35], a[192:195], v[16:19]\n\t"
    "ds_read_b128 v[56:59], %[bA] offset:2304\n\t"
    "s_waitcnt lgkmcnt(7)\n\t"
    "v_mfma_f32_16x16x32_bf16 v[20:23], v[34:37], a[192:195], v[20:23]\n\t"
    "ds_read_b64 v[60:61], %[bA] offset:2320\n\t"
    "v_mfma_f32_16x16x32_bf16 v[24:27], v[32:35], a[128:131], v[24:27]\n\t"
    "ds_read_b128 v[62:65], %[bA] offset:2368\n\t"
    "v_mfma_f32_16x16x32_bf16 v[28:31], v[34:37], a[128:131], v[28:31]\n\t"
    "ds_read_b64 v[66:67], %[bA] offset:2384\n\t"
    "s_waitcnt lgkmcnt(9)\n\t"
    "v_mfma_f32_16x16x32_bf16 v[16:19], v[38:41], a[196:199], v[16:19]\n\t"
    "ds_read_b128 v[68:71], %[bA] offset:2432\n\t"
    "v_fmac_f32 v104, v112, v8\n\t"
    "v_fmac_f32 v105, v113, v9\n\t"
    "s_waitcnt lgkmcnt(9)\n\t"
    "v_mfma_f32_16x16x32_bf16 v[20:23], v[40:43], a[196:199], v[20:23]\n\t"
    "ds_read_b64 v[72:73], %[bA] offset:2448\n\t"
    "v_cvt_pk_bf16_f32 v100, v104, v105\n\t"
    "v_fmac_f32 v106, v114, v10\n\t"
    "v_mfma_f32_16x16x32_bf16 v[24:27], v[38:41], a[132:135], v[24:27]\n\t"
    "v_fmac_f32 v107, v115, v11\n\t"
    "v_cvt_pk_bf16_f32 v101, v106, v107\n\t"
    "v_mfma_f32_16x16x32_bf16 v[28:31], v[40:43], a[132:135], v[28:31]\n\t"
    "v_fmac_f32 v108, v116, v12\n\t"
    "v_fmac_f32 v109, v117, v13\n\t"
    "s_waitcnt lgkmcnt(9)\n\t"
    "v_mfma_f32_16x16x32_bf16 v[16:19], v[44:47], a[200:203], v[16:19]\n\t"
    "ds_read_b128 v[74:77], %[bA] offset:2496\n\t"
    "v_cvt_pk_bf16_f32 v102, v108, v109\n\t"
    "v_fmac_f32 v110, v118, v14\n\t"
    "s_waitcnt lgkmcnt(9)\n\t"
    "v_mfma_f32_16x16x32_bf16 v[20:23], v[46:49], a[200:203], v[20:23]\n\t"
    "ds_read_b64 v[78:79], %[bA] offset:2512\n\t"
    "v_fmac_f32 v111, v119, v15\n\t"
    "v_cvt_pk_bf16_f32 v103, v110, v111\n\t"
    "v_mfma_f32_16x16x32_bf16 v[24:27], v[44:47], a[136:139], v[24:27]\n\t"
    "buffer_store_dwordx4 v[100:103], %[vo], s[44:47], s53 offen sc1\n\t"
    "s_add_u32 s53, s53, 589824\n\t"
    "v_mfma_f32_16x16x32_bf16 v[28:31], v[46:49], a[136:139], v[28:31]\n\t"
    "ds_read_b128 v[80:83], %[uU] offset:10240\n\t"
    "ds_read_b128 v[120:123], %[sU] offset:8704\n\t"
    "s_waitcnt lgkmcnt(11)\n\t"
    "v_mfma_f32_16x16x32_bf16 v[16:19], v[50:53], a[204:207], v[16:19]\n\t"
    "ds_read_b128 v[32:35], %[bA] offset:2560\n\t"
    "ds_read_b128 v[124:127], %[sU] offset:9760\n\t"
    "ds_read_b128 v[128:131], %[sU] offset:10816\n\t"
    "s_waitcnt lgkmcnt(13)\n\t"
    "v_mfma_f32_16x16x32_bf16 v[20:23], v[52:55], a[204:207], v[20:23]\n\t"
    "ds_read_b64 v[36:37], %[bA] offset:2576\n\t"
    "ds_read_b128 v[132:135], %[sU] offset:11872\n\t"
    "v_mfma_f32_16x16x32_bf16 v[24:27], v[50:53], a[140:143], v[24:27]\n\t"
    "v_mfma_f32_16x16x32_bf16 v[28:31], v[52:55], a[140:143], v[28:31]\n\t"
    "s_waitcnt lgkmcnt(0)\n\t"
    "v_mfma_f32_16x16x32_bf16 v[16:19], v[56:59], a[208:211], v[16:19]\n\t"
    "ds_read_b128 v[38:41], %[bA] offset:2624\n\t"
    "v_lshlrev_b32 v104, 16, v80\n\t"
    "v_and_b32 v105, 0xffff0000, v80\n\t"
    "v_mfma_f32_16x16x32_bf16 v[20:23], v[58:61], a[208:211], v[20:23]\n\t"
    "ds_read_b64 v[42:43], %[bA] offset:2640\n\t"
    "v_lshlrev_b32 v106, 16, v81\n\t"
    "v_and_b32 v107, 0xffff0000, v81\n\t"
    "v_mfma_f32_16x16x32_bf16 v[24:27], v[56:59], a[144:147], v[24:27]\n\t"
    "v_lshlrev_b32 v108, 16, v82\n\t"
    "v_and_b32 v109, 0xffff0000, v82\n\t"
    "v_mfma_f32_16x16x32_bf16 v[28:31], v[58:61], a[144:147], v[28:31]\n\t"
    "v_lshlrev_b32 v110, 16, v83\n\t"
    "v_and_b32 v111, 0xffff0000, v83\n\t"
    "v_mfma_f32_16x16x32_bf16 v[16:19], v[62:65], a[212:215], v[16:19]\n\t"
    "ds_read_b128 v[44:47], %[bA] offset:2688\n\t"
    "v_fma_f32 v112, v104, v120, v128\n\t"
    "v_fma_f32 v113, v105, v121, v129\n\t"
    "v_mfma_f32_16x16x32_bf16 v[20:23], v[64:67], a[212:215], v[20:23]\n\t"
    "ds_read_b64 v[48:49], %[bA] offset:2704\n\t"
    "v_fma_f32 v114, v106, v122, v130\n\t"
    "v_fma_f32 v115, v107, v123, v131\n\t"
    "v_mfma_f32_16x16x32_bf16 v[24:27], v[62:65], a[148:151], v[24:27]\n\t"
    "v_fma_f32 v116, v108, v124, v132\n\t"
    "v_fma_f32 v117, v109, v125, v133\n\t"
    "v_mfma_f32_16x16x32_bf16 v[28:31], v[64:67], a[148:151], v[28:31]\n\t"
    "v_fma_f32 v118, v110, v126, v134\n\t"
    "v_fma_f32 v119, v111, v127, v135\n\t"
    "v_mfma_f32_16x16x32_bf16 v[16:19], v[68:71], a[216:219], v[16:19]\n\t"
    "ds_read_b128 v[50:53], %[bA] offset:2752\n\t"
    "v_mfma_f32_16x16x32_bf16 v[20:23], v[70:73], a[216:219], v[20:23]\n\t"
    "ds_read_b64 v[54:55], %[bA] offset:2768\n\t"
    "v_mfma_f32_16x16x32_bf16 v[24:27], v[68:71], a[152:155], v[24:27]\n\t"
    "v_mfma_f32_16x16x32_bf16 v[28:31], v[70:73], a[152:155], v[28:31]\n\t"
    "v_mfma_f32_16x16x32_bf16 v[16:19], v[74:77], a[220:223], v[16:19]\n\t"
    "ds_read_b128 v[56:59], %[bA] offset:2816\n\t"
    "v_mfma_f32_16x16x32_bf16 v[20:23], v[76:79], a[220:223], v[20:23]\n\t"
    "ds_read_b64 v[60:61], %[bA] offset:2832\n\t"
    "v_mfma_f32_16x16x32_bf16 v[24:27], v[74:77], a[156:159], v[24:27]\n\t"
    "v_mfma_f32_16x16x32_bf16 v[28:31], v[76:79], a[156:159], v[28:31]\n\t"
    "v_mfma_f32_16x16x32_bf16 v[16:19], v[32:35], a[224:227], v[16:19]\n\t"
    "ds_read_b128 v[62:65], %[bA] offset:2880\n\t"
    "v_mfma_f32_16x16x32_bf16 v[20:23], v[34:37], a[224:227], v[20:23]\n\t"
    "ds_read_b64 v[66:67], %[bA] offset:2896\n\t"
    "v_mfma_f32_16x16x32_bf16 v[24:27], v[32:35], a[160:163], v[24:27]\n\t"
    "v_mfma_f32_16x16x32_bf16 v[28:31], v[34:37], a[160:163], v[28:31]\n\t"
    "s_waitcnt lgkmcnt(9)\n\t"
    "v_mfma_f32_16x16x32_bf16 v[16:19], v[38:41], a[228:231], v[16:19]\n\t"
    "ds_read_b128 v[68:71], %[bA] offset:2944\n\t"
    "s_waitcnt lgkmcnt(9)\n\t"
    "v_mfma_f32_16x16x32_bf16 v[20:23], v[40:43], a[228:231], v[20:23]\n\t"
    "ds_read_b64 v[72:73], %[bA] offset:2960\n\t"
    "v_mfma_f32_16x16x32_bf16 v[24:27], v[38:41], a[164:167], v[24:27]\n\t"
    "v_mfma_f32_16x16x32_bf16 v[28:31], v[40:43], a[164:167], v[28:31]\n\t"
    "s_waitcnt lgkmcnt(9)\n\t"
    "v_mfma_f32_16x16x32_bf16 v[16:19], v[44:47], a[232:235], v[16:19]\n\t"
    "ds_read_b128 v[74:77], %[bA] offset:3008\n\t"
    "s_waitcnt lgkmcnt(9)\n\t"
    "v_mfma_f32_16x16x32_bf16 v[20:23], v[46:49], a[232:235], v[20:23]\n\t"
    "ds_read_b64 v[78:79], %[bA] offset:3024\n\t"
    "v_mfma_f32_16x16x32_bf16 v[24:27], v[44:47], a[168:171], v[24:27]\n\t"
    "v_mfma_f32_16x16x32_bf16 v[28:31], v[46:49], a[168:171], v[28:31]\n\t"
    "s_waitcnt lgkmcnt(9)\n\t"
    "v_mfma_f32_16x16x32_bf16 v[16:19], v[50:53], a[236:239], v[16:19]\n\t"
    "ds_read_b128 v[32:35], %[bA] offset:3072\n\t"
    "s_waitcnt lgkmcnt(9)\n\t"
    "v_mfma_f32_16x16x32_bf16 v[20:23], v[52:55], a[236:239], v[20:23]\n\t"
    "ds_read_b64 v[36:37], %[bA] offset:3088\n\t"
    "v_mfma_f32_16x16x32_bf16 v[24:27], v[50:53], a[172:175], v[24:27]\n\t"
    "v_mfma_f32_16x16x32_bf16 v[28:31], v[52:55], a[172:175], v[28:31]\n\t"
    "s_waitcnt lgkmcnt(9)\n\t"
    "v_mfma_f32_16x16x32_bf16 v[16:19], v[56:59], a[240:243], v[16:19]\n\t"
    "ds_read_b128 v[38:41], %[bA] offset:3136\n\t"
    "s_waitcnt lgkmcnt(9)\n\t"
    "v_mfma_f32_16x16x32_bf16 v[20:23], v[58:61], a[240:243], v[20:23]\n\t"
    "ds_read_b64 v[42:43], %[bA] offset:3152\n\t"
    "v_mfma_f32_16x16x32_bf16 v[24:27], v[56:59], a[176:179], v[24:27]\n\t"
    "v_mfma_f32_16x16x32_bf16 v[28:31], v[58:61], a[176:179], v[28:31]\n\t"
    "s_waitcnt lgkmcnt(9)\n\t"
    "v_mfma_f32_16x16x32_bf16 v[16:19], v[62:65], a[244:247], v[16:19]\n\t"
    "ds_read_b128 v[44:47], %[bA] offset:3200\n\t"
    "s_waitcnt lgkmcnt(9)\n\t"
    "v_mfma_f32_16x16x32_bf16 v[20:23], v[64:67], a[244:247], v[20:23]\n\t"
    "ds_read_b64 v[48:49], %[bA] offset:3216\n\t"
    "v_mfma_f32_16x16x32_bf16 v[24:27], v[62:65], a[180:183], v[24:27]\n\t"
    "v_mfma_f32_16x16x32_bf16 v[28:31], v[64:67], a[180:183], v[28:31]\n\t"
    "s_waitcnt lgkmcnt(9)\n\t"
    "v_mfma_f32_16x16x32_bf16 v[16:19], v[68:71], a[248:251], v[16:19]\n\t"
    "ds_read_b128 v[50:53], %[bA] offset:3264\n\t"
    "s_waitcnt lgkmcnt(9)\n\t"
    "v_mfma_f32_16x16x32_bf16 v[20:23], v[70:73], a[248:251], v[20:23]\n\t"
    "ds_read_b64 v[54:55], %[bA] offset:3280\n\t"
    "v_mfma_f32_16x16x32_bf16 v[24:27], v[68:71], a[184:187], v[24:27]\n\t"
    "v_mfma_f32_16x16x32_bf16 v[28:31], v[70:73], a[184:187], v[28:31]\n\t"
    "s_waitcnt lgkmcnt(9)\n\t"
    "v_mfma_f32_16x16x32_bf16 v[16:19], v[74:77], a[252:255], v[16:19]\n\t"
    "ds_read_b128 v[56:59], %[bA] offset:3328\n\t"
    "s_waitcnt lgkmcnt(9)\n\t"
    "v_mfma_f32_16x16x32_bf16 v[20:23], v[76:79], a[252:255], v[20:23]\n\t"
    "ds_read_b64 v[60:61], %[bA] offset:3344\n\t"
    "v_mfma_f32_16x16x32_bf16 v[24:27], v[74:77], a[188:191], v[24:27]\n\t"
    "v_mfma_f32_16x16x32_bf16 v[28:31], v[76:79], a[188:191], v[28:31]\n\t"
    "s_waitcnt lgkmcnt(9)\n\t"
    "v_mfma_f32_16x16x32_bf16 v[24:27], v[32:35], a[192:195], v[24:27]\n\t"
    "ds_read_b128 v[62:65], %[bA] offset:3392\n\t"
    "s_waitcnt lgkmcnt(9)\n\t"
    "v_mfma_f32_16x16x32_bf16 v[28:31], v[34:37], a[192:195], v[28:31]\n\t"
    "ds_read_b64 v[66:67], %[bA] offset:3408\n\t"
    "s_waitcnt lgkmcnt(9)\n\t"
    "v_mfma_f32_16x16x32_bf16 v[24:27], v[38:41], a[196:199], v[24:27]\n\t"
    "ds_read_b128 v[68:71], %[bA] offset:3456\n\t"
    "s_waitcnt lgkmcnt(9)\n\t"
    "v_mfma_f32_16x16x32_bf16 v[28:31], v[40:43], a[196:199], v[28:31]\n\t"
    "ds_read_b64 v[72:73], %[bA] offset:3472\n\t"
    "global_load_dword %[wfl], %[gwo], %[pG] sc1\n\t"
    "s_waitcnt lgkmcnt(9)\n\t"
    "v_mfma_f32_16x16x32_bf16 v[24:27], v[44:47], a[200:203], v[24:27]\n\t"
    "ds_read_b128 v[74:77], %[bA] offset:3520\n\t"
    "v_fmac_f32 v104, v112, v16\n\t"
    "v_fmac_f32 v105, v113, v17\n\t"
    "s_waitcnt lgkmcnt(9)\n\t"
    "v_mfma_f32_16x16x32_bf16 v[28:31], v[46:49], a[200:203], v[28:31]\n\t"
    "ds_read_b64 v[78:79], %[bA] offset:3536\n\t"
    "v_cvt_pk_bf16_f32 v100, v104, v105\n\t"
    "v_fmac_f32 v106, v114, v18\n\t"
    "s_waitcnt lgkmcnt(9)\n\t"
    "v_mfma_f32_16x16x32_bf16 v[24:27], v[50:53], a[204:207], v[24:27]\n\t"
    "ds_read_b128 v[32:35], %[bA] offset:3584\n\t"
    "v_fmac_f32 v107, v115, v19\n\t"
    "v_cvt_pk_bf16_f32 v101, v106, v107\n\t"
    "s_waitcnt lgkmcnt(9)\n\t"
    "v_mfma_f32_16x16x32_bf16 v[28:31], v[52:55], a[204:207], v[28:31]\n\t"
    "ds_read_b64 v[36:37], %[bA] offset:3600\n\t"
    "v_fmac_f32 v108, v116, v20\n\t"
    "v_fmac_f32 v109, v117, v21\n\t"
    "s_waitcnt lgkmcnt(9)\n\t"
    "v_mfma_f32_16x16x32_bf16 v[24:27], v[56:59], a[208:211], v[24:27]\n\t"
    "ds_read_b128 v[38:41], %[bA] offset:3648\n\t"
    "v_cvt_pk_bf16_f32 v102, v108, v109\n\t"
    "v_fmac_f32 v110, v118, v22\n\t"
    "s_waitcnt lgkmcnt(9)\n\t"
    "v_mfma_f32_16x16x32_bf16 v[28:31], v[58:61], a[208:211], v[28:31]\n\t"
    "ds_read_b64 v[42:43], %[bA] offset:3664\n\t"
    "v_fmac_f32 v111, v119, v23\n\t"
    "v_cvt_pk_bf16_f32 v103, v110, v111\n\t"
    "s_waitcnt lgkmcnt(9)\n\t"
    "v_mfma_f32_16x16x32_bf16 v[24:27], v[62:65], a[212:215], v[24:27]\n\t"
    "ds_read_b128 v[44:47], %[bA] offset:3712\n\t"
    "buffer_store_dwordx4 v[100:103], %[vo], s[44:47], s53 offen sc1\n\t"
    "s_add_u32 s53, s53, 589824\n\t"
    "s_waitcnt lgkmcnt(9)\n\t"
    "v_mfma_f32_16x16x32_bf16 v[28:31], v[64:67], a[212:215], v[28:31]\n\t"
    "ds_read_b64 v[48:49], %[bA] offset:3728\n\t"
    "ds_read_b128 v[80:83], %[uU] offset:15360\n\t"
    "ds_read_b128 v[120:123], %[sU] offset:13056\n\t"
    "s_waitcnt lgkmcnt(11)\n\t"
    "v_mfma_f32_16x16x32_bf16 v[24:27], v[68:71], a[216:219], v[24:27]\n\t"
    "ds_read_b128 v[50:53], %[bA] offset:3776\n\t"
    "ds_read_b128 v[124:127], %[sU] offset:14112\n\t"
    "ds_read_b128 v[128:131], %[sU] offset:15168\n\t"
    "s_waitcnt lgkmcnt(13)\n\t"
    "v_mfma_f32_16x16x32_bf16 v[28:31], v[70:73], a[216:219], v[28:31]\n\t"
    "ds_read_b64 v[54:55], %[bA] offset:3792\n\t"
    "ds_read_b128 v[132:135], %[sU] offset:16224\n\t"
    "s_waitcnt lgkmcnt(14)\n\t"
    "v_mfma_f32_16x16x32_bf16 v[24:27], v[74:77], a[220:223], v[24:27]\n\t"
    "ds_read_b128 v[56:59], %[bA] offset:3840\n\t"
    "s_waitcnt lgkmcnt(14)\n\t"
    "v_mfma_f32_16x16x32_bf16 v[28:31], v[76:79], a[220:223], v[28:31]\n\t"
    "ds_read_b64 v[60:61], %[bA] offset:3856\n\t"
    "s_waitcnt lgkmcnt(2)\n\t"
    "v_mfma_f32_16x16x32_bf16 v[24:27], v[32:35], a[224:227], v[24:27]\n\t"
    "ds_read_b128 v[62:65], %[bA] offset:3904\n\t"
    "v_lshlrev_b32 v104, 16, v80\n\t"
    "v_and_b32 v105, 0xffff0000, v80\n\t"
    "v_mfma_f32_16x16x32_bf16 v[28:31], v[34:37], a[224:227], v[28:31]\n\t"
    "ds_read_b64 v[66:67], %[bA] offset:3920\n\t"
    "v_lshlrev_b32 v106, 16, v81\n\t"
    "v_and_b32 v107, 0xffff0000, v81\n\t"
    "v_mfma_f32_16x16x32_bf16 v[24:27], v[38:41], a[228:231], v[24:27]\n\t"
    "ds_read_b128 v[68:71], %[bA] offset:3968\n\t"
    "v_lshlrev_b32 v108, 16, v82\n\t"
    "v_and_b32 v109, 0xffff0000, v82\n\t"
    "v_mfma_f32_16x16x32_bf16 v[28:31], v[40:43], a[228:231], v[28:31]\n\t"
    "ds_read_b64 v[72:73], %[bA] offset:3984\n\t"
    "v_lshlrev_b32 v110, 16, v83\n\t"
    "v_and_b32 v111, 0xffff0000, v83\n\t"
    "v_mfma_f32_16x16x32_bf16 v[24:27], v[44:47], a[232:235], v[24:27]\n\t"
    "ds_read_b128 v[74:77], %[bA] offset:4032\n\t"
    "v_fma_f32 v112, v104, v120, v128\n\t"
    "v_fma_f32 v113, v105, v121, v129\n\t"
    "v_mfma_f32_16x16x32_bf16 v[28:31], v[46:49], a[232:235], v[28:31]\n\t"
    "ds_read_b64 v[78:79], %[bA] offset:4048\n\t"
    "v_fma_f32 v114, v106, v122, v130\n\t"
    "v_fma_f32 v115, v107, v123, v131\n\t"
    "v_mfma_f32_16x16x32_bf16 v[24:27], v[50:53], a[236:239], v[24:27]\n\t"
    "v_fma_f32 v116, v108, v124, v132\n\t"
    "v_fma_f32 v117, v109, v125, v133\n\t"
    "v_mfma_f32_16x16x32_bf16 v[28:31], v[52:55], a[236:239], v[28:31]\n\t"
    "v_fma_f32 v118, v110, v126, v134\n\t"
    "v_fma_f32 v119, v111, v127, v135\n\t"
    "s_waitcnt lgkmcnt(7)\n\t"
    "v_mfma_f32_16x16x32_bf16 v[24:27], v[56:59], a[240:243], v[24:27]\n\t"
    "s_waitcnt lgkmcnt(6)\n\t"
    "v_mfma_f32_16x16x32_bf16 v[28:31], v[58:61], a[240:243], v[28:31]\n\t"
    "s_waitcnt lgkmcnt(5)\n\t"
    "v_mfma_f32_16x16x32_bf16 v[24:27], v[62:65], a[244:247], v[24:27]\n\t"
    "s_waitcnt lgkmcnt(4)\n\t"
    "v_mfma_f32_16x16x32_bf16 v[28:31], v[64:67], a[244:247], v[28:31]\n\t"
    "s_waitcnt lgkmcnt(3)\n\t"
    "v_mfma_f32_16x16x32_bf16 v[24:27], v[68:71], a[248:251], v[24:27]\n\t"
    "s_waitcnt lgkmcnt(2)\n\t"
    "v_mfma_f32_16x16x32_bf16 v[28:31], v[70:73], a[248:251], v[28:31]\n\t"
    "s_waitcnt lgkmcnt(1)\n\t"
    "v_mfma_f32_16x16x32_bf16 v[24:27], v[74:77], a[252:255], v[24:27]\n\t"
    "s_waitcnt lgkmcnt(0)\n\t"
    "v_mfma_f32_16x16x32_bf16 v[28:31], v[76:79], a[252:255], v[28:31]\n\t"
    "s_nop 7\n\t"
    "v_fmac_f32 v104, v112, v24\n\t"
    "v_fmac_f32 v105, v113, v25\n\t"
    "v_cvt_pk_bf16_f32 v100, v104, v105\n\t"
    "v_fmac_f32 v106, v114, v26\n\t"
    "v_fmac_f32 v107, v115, v27\n\t"
    "v_cvt_pk_bf16_f32 v101, v106, v107\n\t"
    "v_fmac_f32 v108, v116, v28\n\t"
    "v_fmac_f32 v109, v117, v29\n\t"
    "v_cvt_pk_bf16_f32 v102, v108, v109\n\t"
    "v_fmac_f32 v110, v118, v30\n\t"
    "v_fmac_f32 v111, v119, v31\n\t"
    "v_cvt_pk_bf16_f32 v103, v110, v111\n\t"
    "buffer_store_dwordx4 v[100:103], %[vo], s[44:47], s53 offen sc1\n\t"
    "s_add_u32 s53, s53, 589824\n\t"
    "s_waitcnt vmcnt(0) lgkmcnt(0)\n\t"
            : [wfl] "=&v"(wfl)
            : [vx] "v"(vx),  [bA] "v"(bA), [bX] "v"(bX),
              [uU] "v"(uU), [uw] "v"(uw), [sU] "v"(sU), [sw] "v"(sw), [sv] "v"(sv), [vo] "v"(vo), [ch] "s"(ch), [us] "s"(us),
              [pO] "s"(hT), [pU] "s"(QT), [pG] "s"(pG), [gwo] "v"(gwo), [guo] "v"(guo), [pC] "s"(cnt), [wid] "s"(w), [ds0] "v"(ds0), [ds1] "v"(ds1)
            : "v0", "v1", "v2", "v3", "v4", "v5", "v6", "v7", "v8", "v9", "v10", "v11", "v12", "v13", "v14", "v15", "v16", "v17", "v18", "v19", "v20", "v21", "v22", "v23",
      "v24", "v25", "v26", "v27", "v28", "v29", "v30", "v31", "v32", "v33", "v34", "v35", "v36", "v37", "v38", "v39", "v40", "v41", "v42", "v43", "v44", "v45", "v46", "v47",
      "v48", "v49", "v50", "v51", "v52", "v53", "v54", "v55", "v56", "v57", "v58", "v59", "v60", "v61", "v62", "v63", "v64", "v65", "v66", "v67", "v68", "v69", "v70", "v71",
      "v72", "v73", "v74", "v75", "v76", "v77", "v78", "v79", "v80", "v81", "v82", "v83", "v84", "v85", "v86", "v87", "v88", "v89", "v90", "v91", "v92", "v93", "v94", "v95",
      "v96", "v97", "v98", "v99", "v100", "v101", "v102", "v103", "v104", "v105", "v106", "v107", "v108", "v109", "v110", "v111", "v112", "v113", "v114", "v115", "v116", "v117", "v118", "v119",
      "v120", "v121", "v122", "v123", "v124", "v125", "v126", "v127", "v128", "v129", "v130", "v131", "v132", "v133", "v134", "v135", "v136", "v137", "v138", "v139", "v140", "v141", "v142", "v143",
      "v144", "v145", "v146", "v147", "v148", "v149", "v150", "v151", "v152", "v153", "v154", "v155", "v156", "v157", "v158", "v159", "v160", "v161", "v162", "v163", "a0", "a1", "a2", "a3",
      "a4", "a5", "a6", "a7", "a8", "a9", "a10", "a11", "a12", "a13", "a14", "a15", "a16", "a17", "a18", "a19", "a20", "a21", "a22", "a23", "a24", "a25", "a26", "a27",
      "a28", "a29", "a30", "a31", "a32", "a33", "a34", "a35", "a36", "a37", "a38", "a39", "a40", "a41", "a42", "a43", "a44", "a45", "a46", "a47", "a48", "a49", "a50", "a51",
      "a52", "a53", "a54", "a55", "a56", "a57", "a58", "a59", "a60", "a61", "a62", "a63", "a64", "a65", "a66", "a67", "a68", "a69", "a70", "a71", "a72", "a73", "a74", "a75",
      "a76", "a77", "a78", "a79", "a80", "a81", "a82", "a83", "a84", "a85", "a86", "a87", "a88", "a89", "a90", "a91", "a92", "a93", "a94", "a95", "a96", "a97", "a98", "a99",
      "a100", "a101", "a102", "a103", "a104", "a105", "a106", "a107", "a108", "a109", "a110", "a111", "a112", "a113", "a114", "a115", "a116", "a117", "a118", "a119", "a120", "a121", "a122", "a123",
      "a124", "a125", "a126", "a127", "a128", "a129", "a130", "a131", "a132", "a133", "a134", "a135", "a136", "a137", "a138", "a139", "a140", "a141", "a142", "a143", "a144", "a145", "a146", "a147",
      "a148", "a149", "a150", "a151", "a152", "a153", "a154", "a155", "a156", "a157", "a158", "a159", "a160", "a161", "a162", "a163", "a164", "a165", "a166", "a167", "a168", "a169", "a170", "a171",
      "a172", "a173", "a174", "a175", "a176", "a177", "a178", "a179", "a180", "a181", "a182", "a183", "a184", "a185", "a186", "a187", "a188", "a189", "a190", "a191", "a192", "a193", "a194", "a195",
      "a196", "a197", "a198", "a199", "a200", "a201", "a202", "a203", "a204", "a205", "a206", "a207", "a208", "a209", "a210", "a211", "a212", "a213", "a214", "a215", "a216", "a217", "a218", "a219",
      "a220", "a221", "a222", "a223", "a224", "a225", "a226", "a227", "a228", "a229", "a230", "a231", "a232", "a233", "a234", "a235", "a236", "a237", "a238", "a239", "a240", "a241", "a242", "a243",
      "a244", "a245", "a246", "a247", "a248", "a249", "a250", "a251", "a252", "a253", "a254", "a255", "s36", "s37", "s38", "s39", "s40", "s41", "s42", "s43", "s44", "s45", "s46", "s47",
      "s48", "s49", "s50", "s51", "s52", "s53", "s54", "s55", "s56", "s57", "vcc", "scc", "memory");
            if (tid == 0) *(volatile LAS unsigned*)(LP + PREP_OFF) = (wfl == tag);
            asm volatile("s_waitcnt lgkmcnt(0)" ::: "memory");
            __builtin_amdgcn_s_barrier();
            if (w == 1) arrive(32 + (xa & (SHH - 1)), 144 / SHH, 2, xa & (SHH - 1));
            wok = __builtin_amdgcn_readfirstlane(*(volatile LAS unsigned*)(LP + PREP_OFF));
        } else {
            const __amdgpu_buffer_rsrc_t ro = __builtin_amdgcn_make_buffer_rsrc((void*)((char*)wstream + (size_t)xa * (4 * 324 * 1024)), 0, 4 * 324 * 1024, 0x00020000);
            const __amdgpu_buffer_rsrc_t rc = __builtin_amdgcn_make_buffer_rsrc((void*)csum, 0, 3 * F * 4, 0x00020000);
            w2dma();
            if (w == 3) asm volatile("s_waitcnt vmcnt(12)" ::: "memory");
            __builtin_amdgcn_s_barrier();
            asm volatile("" ::: "memory");
            const bf16x8 ones = {0x3F80, 0x3F80, 0x3F80, 0x3F80, 0x3F80, 0x3F80, 0x3F80, 0x3F80};
#pragma unroll
            for (int n = 0; n < 2; ++n) {

                asm volatile("" ::: "memory");
                const int u1 = 2 * (li - 18) + n, cgp = u1 % 9, kt = u1 / 9, T = 4 * cgp + w;
                const unsigned drop = u1 < 27 ? 0u : 0x40000000u;
                LAS char* rd = LP + n * 24576 + (8 * g + q4) * 128 + ((w ^ sigB) * 32) + p4 * 8;
                f32x4 cs = {0.f, 0.f, 0.f, 0.f};
#pragma unroll
                for (int i = 0; i < 6; ++i) {
                    const bf16x8 a = tr_frag(rd + i * 4096, 512);
                    cs = __builtin_amdgcn_mfma_f32_16x16x32_bf16(a, ones, cs, 0, 0, 0);
                    __builtin_amdgcn_raw_buffer_store_b128(__builtin_bit_cast(u32x4, a), ro, (((T / 9) * 324 + (6 * kt + i) * 9 + T % 9) * 1024 + lane * 16) | drop, 0, 16);
                }
                __builtin_amdgcn_raw_buffer_store_b128(__builtin_bit_cast(u32x4, cs), rc, r == 0 ? ((unsigned)(kt * F + 576 * xa + 16 * T + 4 * g) * 4) | drop : 0x7fffff00u, 0, 16);
                asm volatile("" ::: "memory");
            }
            asm volatile("s_waitcnt vmcnt(14)" ::: "memory");
            asm volatile("" ::: "memory");
#pragma unroll
            for (int n = 0; n < 2; ++n) {
                const int u2 = (li - 18) * 8 + n * 4 + w, js = u2 / 6, cq = u2 % 6;
                const unsigned drop = u2 < 108 ? 0u : 0x40000000u;
                LAS char* rd0 = LP + 49152 + (n * 4 + w) * 6144 + (8 * g + q4) * 192 + p4 * 8;
#pragma unroll
                for (int i = 0; i < 6; ++i) {
                    const int T = 6 * cq + i, G = T >> 2, wc = T & 3;
                    const int slot = G == 8 ? 0 : G == xa ? 8 : ((G - xa) & 7);
                    const bf16x8 a = tr_frag(rd0 + i * 32, 4 * 192);
                    __builtin_amdgcn_raw_buffer_store_b128(__builtin_bit_cast(u32x4, a), ro, ((wc * 324 + 162 + slot * 18 + js) * 1024 + lane * 16) | drop, 0, 16);
                }
                asm volatile("" ::: "memory");
            }
            asm volatile("s_waitcnt vmcnt(0)" ::: "memory");
            __builtin_amdgcn_s_barrier();
            if (w == 1) arrive(20 + xa, 14, 3, xa);
        }
    }
    unsigned hb[4], Hw[4], ra[4];
    const int sigR = ((q4 >> 1) & 1) | (((g >> 1) & 1) << 1);
#pragma unroll
    for (int m = 0; m < 4; ++m) {
        hb[m] = L + HT_OFF + (8 * g + q4) * 128 + ((((m + w) & 3) ^ sigB) * 32) + p4 * 8;
        Hw[m] = L + H_OFF + ((m + w) & 3) * 18432 + w * (9 * 512) + (g >> 1) * 256 + r * 16 + (g & 1) * 8;
        ra[m] = L + HT_OFF + (64 * fs + 16 * w + 4 * g + q4) * 128 + ((m ^ sigR) * 32) + p4 * 8;
    }
    const int smt = 2 * (fs & 1) + (g & 1), sn0 = 512 + 16 * (fs >> 1) + 4 * w;
    const unsigned sa0 = L + HT_OFF + (sn0 + q4) * 128 + ((smt ^ (((q4 >> 1) & 1) | (((w >> 1) & 1) << 1))) * 32) + p4 * 8;
    const unsigned shv = g * 1024 + w * 256 + r * 16;
    const int krow = lane >> 3, sl = lane & 7;
    const int sigW = ((krow >> 1) & 1) | ((w & 1) << 1);
    unsigned hwr = L + HT_OFF + w * 1024 + krow * 128 + (((sl >> 1) ^ sigW) * 32) + (sl & 1) * 16;
    const unsigned stw = L + ST_OFF + (16 * w + r) * 4;
    const unsigned csr = L + CS_OFF + w * 1024 + g * 16, csb = L + CS_OFF + w * 1024;
    const unsigned hrb = L + H_OFF;
    const unsigned rso = L + RS_OFF + ((g == q4) ? (unsigned)(16 * w + r) * 4 : 256u + lane * 4);
    const unsigned rsr = L + RS_OFF + r * 4, srs = rsr + smt * 64;
    const unsigned oo = r * 1152 + (64 * fs + 16 * w + 4 * g) * 2;
    const unsigned so = g < 2 ? (unsigned)((16 * smt + r) * 1152 + sn0 * 2) : 0x7fffff00u;
    const unsigned one = (g == q4) ? ((p4 & 1) ? 0x3F800000u : 0x00003F80u) : 0u;
    const u32x4 sel0 = {(p4 >> 1) ? 0u : one, (p4 >> 1) ? one : 0u, 0u, 0u};
    const u32x4 sel1 = {0u, 0u, (p4 >> 1) ? 0u : one, (p4 >> 1) ? one : 0u};
    const unsigned on2 = (r & 1) ? 0x3F800000u : 0x00003F80u;
    const u32x4 sel2 = {(r >> 1) == 0 ? on2 : 0u, (r >> 1) == 1 ? on2 : 0u, (r >> 1) == 2 ? on2 : 0u, (r >> 1) == 3 ? on2 : 0u};
    const char* pW = (const char*)wstream + (size_t)(fs * 4 + w) * (324 * 1024);
    const char* pH = (const char*)hT + (size_t)mtile * 73728 + w * 1024;
    const char* pP = part + (size_t)mtile * 65536;
    const char* pC = (const char*)(csum + 576 * fs + 144 * w);
    const char* pR = (const char*)(cnt + mtile * 256);
    const char* pO = (const char*)(out + (size_t)mtile * 64 * D);
    const int w2k = w * 2048, w32 = w * 32, cb = fs * 128 + w * 32, rb = fs * 8192 + w * 2048;
    asm volatile(
    "s_mov_b64 s[36:37], %[pW]\n\t"
    "s_mov_b32 s38, 331776\n\t"
    "s_mov_b32 s39, 0x20000\n\t"
    "s_mov_b64 s[40:41], %[pH]\n\t"
    "s_mov_b32 s42, 73728\n\t"
    "s_mov_b32 s43, 0x20000\n\t"
    "s_mov_b64 s[44:45], %[pP]\n\t"
    "s_mov_b32 s46, 14745600\n\t"
    "s_mov_b32 s47, 0x20000\n\t"
    "s_mov_b64 s[48:49], %[pC]\n\t"
    "s_mov_b32 s50, 37888\n\t"
    "s_mov_b32 s51, 0x20000\n\t"
    "s_mov_b64 s[52:53], %[pR]\n\t"
    "s_mov_b32 s54, 1024\n\t"
    "s_mov_b32 s55, 0x20000\n\t"
    "s_mov_b32 s56, 0\n\t"
    "s_mov_b32 s57, 0\n\t"
    "s_cmp_lg_u32 %[wok], 0\n\t"
    "s_cbranch_scc1 L_gwskip_%=\n\t"
    "s_cmp_lg_u32 %[wid], 0\n\t"
    "s_cbranch_scc1 L_gwok_%=\n\t"
    "s_mov_b32 s61, 0x20000\n\t"
    "v_mov_b32 v205, %[gws]\n\t"
    "L_gw_%=:\n\t"
    "global_load_dword v204, v205, %[pG] offset:0 sc1\n\t"
    "s_waitcnt vmcnt(0)\n\t"
    "v_cmp_ne_u32 vcc, %[tag], v204\n\t"
    "s_sub_u32 s61, s61, 1\n\t"
    "s_cmp_eq_u64 vcc, 0\n\t"
    "s_cbranch_scc1 L_gwok_%=\n\t"
    "s_cmp_eq_u32 s61, 0\n\t"
    "s_cbranch_scc1 L_gwok_%=\n\t"
    "s_sleep 4\n\t"
    "s_branch L_gw_%=\n\t"
    "L_gwok_%=:\n\t"
    "s_barrier\n\t"
    "L_gwskip_%=:\n\t"
    "s_movk_i32 s59, 18432\n\t"
    "s_mov_b32 s60, 36864\n\t"
    "buffer_load_dwordx4 v[160:163], %[voff], s[48:51], 0 offen sc1\n\t"
    "buffer_load_dwordx4 v[164:167], %[voff], s[48:51], s59 offen sc1\n\t"
    "buffer_load_dwordx4 v[168:171], %[voff], s[48:51], s60 offen sc1\n\t"
    "buffer_load_dwordx4 a[72:75], %[voff], s[36:39], s56 offen offset:0 sc1\n\t"
    "v_mov_b32 v200, 0x3f803f80\n\t"
    "v_mov_b32 v201, 0x3f803f80\n\t"
    "v_mov_b32 v202, 0x3f803f80\n\t"
    "v_mov_b32 v203, 0x3f803f80\n\t"
    "buffer_load_dwordx4 a[76:79], %[voff], s[36:39], s56 offen offset:1024 sc1\n\t"
    "buffer_load_dwordx4 a[80:83], %[voff], s[36:39], s56 offen offset:2048 sc1\n\t"
    "buffer_load_dwordx4 a[84:87], %[voff], s[36:39], s56 offen offset:3072 sc1\n\t"
    "s_add_u32 s56, s56, 4096\n\t"
    "buffer_load_dwordx4 a[88:91], %[voff], s[36:39], s56 offen offset:0 sc1\n\t"
    "buffer_load_dwordx4 a[92:95], %[voff], s[36:39], s56 offen offset:1024 sc1\n\t"
    "buffer_load_dwordx4 a[96:99], %[voff], s[36:39], s56 offen offset:2048 sc1\n\t"
    "buffer_load_dwordx4 a[100:103], %[voff], s[36:39], s56 offen offset:3072 sc1\n\t"
    "s_add_u32 s56, s56, 4096\n\t"
    "buffer_load_dwordx4 a[104:107], %[voff], s[36:39], s56 offen offset:0 sc1\n\t"
    "buffer_load_dwordx4 a[108:111], %[voff], s[36:39], s56 offen offset:1024 sc1\n\t"
    "buffer_load_dwordx4 a[112:115], %[voff], s[36:39], s56 offen offset:2048 sc1\n\t"
    "buffer_load_dwordx4 a[116:119], %[voff], s[36:39], s56 offen offset:3072 sc1\n\t"
    "s_add_u32 s56, s56, 4096\n\t"
    "buffer_load_dwordx4 a[120:123], %[voff], s[36:39], s56 offen offset:0 sc1\n\t"
    "buffer_load_dwordx4 a[124:127], %[voff], s[36:39], s56 offen offset:1024 sc1\n\t"
    "buffer_load_dwordx4 a[128:131], %[voff], s[36:39], s56 offen offset:2048 sc1\n\t"
    "buffer_load_dwordx4 a[132:135], %[voff], s[36:39], s56 offen offset:3072 sc1\n\t"
    "s_add_u32 s56, s56, 4096\n\t"
    "buffer_load_dwordx4 a[136:139], %[voff], s[36:39], s56 offen offset:0 sc1\n\t"
    "buffer_load_dwordx4 a[140:143], %[voff], s[36:39], s56 offen offset:1024 sc1\n\t"
    "buffer_load_dwordx4 a[0:3], %[voff], s[36:39], s56 offen offset:2048 sc1\n\t"
    "buffer_load_dwordx4 a[4:7], %[voff], s[36:39], s56 offen offset:3072 sc1\n\t"
    "s_add_u32 s56, s56, 4096\n\t"
    "buffer_load_dwordx4 a[8:11], %[voff], s[36:39], s56 offen offset:0 sc1\n\t"
    "buffer_load_dwordx4 a[12:15], %[voff], s[36:39], s56 offen offset:1024 sc1\n\t"
    "buffer_load_dwordx4 a[16:19], %[voff], s[36:39], s56 offen offset:2048 sc1\n\t"
    "buffer_load_dwordx4 a[20:23], %[voff], s[36:39], s56 offen offset:3072 sc1\n\t"
    "s_add_u32 s56, s56, 4096\n\t"
    "buffer_load_dwordx4 a[24:27], %[voff], s[36:39], s56 offen offset:0 sc1\n\t"
    "buffer_load_dwordx4 a[28:31], %[voff], s[36:39], s56 offen offset:1024 sc1\n\t"
    "buffer_load_dwordx4 a[32:35], %[voff], s[36:39], s56 offen offset:2048 sc1\n\t"
    "buffer_load_dwordx4 a[36:39], %[voff], s[36:39], s56 offen offset:3072 sc1\n\t"
    "s_add_u32 s56, s56, 4096\n\t"
    "buffer_load_dwordx4 a[40:43], %[voff], s[36:39], s56 offen offset:0 sc1\n\t"
    "buffer_load_dwordx4 a[44:47], %[voff], s[36:39], s56 offen offset:1024 sc1\n\t"
    "s_add_u32 s56, s56, 2048\n\t"
    "s_waitcnt vmcnt(30)\n\t"
    "v_add_f32 v160, v160, v164\n\t"
    "v_add_f32 v161, v161, v165\n\t"
    "v_add_f32 v162, v162, v166\n\t"
    "v_add_f32 v163, v163, v167\n\t"
    "v_add_f32 v160, v160, v168\n\t"
    "v_add_f32 v161, v161, v169\n\t"
    "v_add_f32 v162, v162, v170\n\t"
    "v_add_f32 v163, v163, v171\n\t"
    "v_add_u32 v164, %[csb], %[voff]\n\t"
    "ds_write_b128 v164, v[160:163]\n\t"
    "s_cmp_lg_u32 %[wid], 0\n\t"
    "s_cbranch_scc1 L_ghok_%=\n\t"
    "s_mov_b32 s61, 0x20000\n\t"
    "L_gh_%=:\n\t"
    "global_load_dword v204, %[gho], %[pG] offset:0 sc1\n\t"
    "s_waitcnt vmcnt(0)\n\t"
    "v_cmp_ne_u32 vcc, %[tag], v204\n\t"
    "s_sub_u32 s61, s61, 1\n\t"
    "s_cmp_eq_u64 vcc, 0\n\t"
    "s_cbranch_scc1 L_ghok_%=\n\t"
    "s_cmp_eq_u32 s61, 0\n\t"
    "s_cbranch_scc1 L_ghok_%=\n\t"
    "s_sleep 4\n\t"
    "s_branch L_gh_%=\n\t"
    "L_ghok_%=:\n\t"
    "s_barrier\n\t"
    "buffer_load_dwordx4 a[156:159], %[voff], s[40:43], s57 offen sc1\n\t"
    "s_add_u32 s57, s57, 0x1000\n\t"
    "buffer_load_dwordx4 a[160:163], %[voff], s[40:43], s57 offen sc1\n\t"
    "s_add_u32 s57, s57, 0x1000\n\t"
    "buffer_load_dwordx4 a[164:167], %[voff], s[40:43], s57 offen sc1\n\t"
    "s_add_u32 s57, s57, 0x1000\n\t"
    "buffer_load_dwordx4 a[144:147], %[voff], s[40:43], s57 offen sc1\n\t"
    "s_add_u32 s57, s57, 0x1000\n\t"
    "buffer_load_dwordx4 a[148:151], %[voff], s[40:43], s57 offen sc1\n\t"
    "s_add_u32 s57, s57, 0x1000\n\t"
    "buffer_load_dwordx4 a[152:155], %[voff], s[40:43], s57 offen sc1\n\t"
    "s_add_u32 s57, s57, 0x1000\n\t"
    "s_waitcnt vmcnt(5)\n\t"
    "ds_write_b128 %[hwr], a[156:159] offset:0\n\t"
    "s_waitcnt vmcnt(4)\n\t"
    "ds_write_b128 %[hwr], a[160:163] offset:4096\n\t"
    "s_waitcnt vmcnt(3)\n\t"
    "ds_write_b128 %[hwr], a[164:167] offset:8192\n\t"
    "v_add_u32 %[hwr], 0x3000, %[hwr]\n\t"
    "s_waitcnt lgkmcnt(0)\n\t"
    "s_barrier\n\t"
    "ds_read_b64_tr_b16 v[144:145], %[hb0]\n\t"
    "ds_read_b64_tr_b16 v[146:147], %[hb0] offset:512\n\t"
    "ds_read_b64_tr_b16 v[148:149], %[hb1]\n\t"
    "ds_read_b64_tr_b16 v[150:151], %[hb1] offset:512\n\t"
    "ds_read_b64_tr_b16 v[152:153], %[hb2]\n\t"
    "ds_read_b64_tr_b16 v[154:155], %[hb2] offset:512\n\t"
    "ds_read_b64_tr_b16 v[156:157], %[hb3]\n\t"
    "ds_read_b64_tr_b16 v[158:159], %[hb3] offset:512\n\t"
    "s_mov_b32 s59, 6\n\t"
    "s_waitcnt lgkmcnt(0)\n\t"
    "v_mfma_f32_16x16x32_bf16 v[192:195], v[200:203], v[144:147], 0\n\t"
    "v_mfma_f32_16x16x32_bf16 v[196:199], v[144:147], v[144:147], 0\n\t"
    "s_waitcnt vmcnt(35)\n\t"
    "v_mfma_f32_16x16x32_bf16 v[0:3], a[72:75], v[144:147], 0\n\t"
    "ds_read_b64_tr_b16 v[160:161], %[hb0] offset:4096\n\t"
    "v_mfma_f32_16x16x32_bf16 v[4:7], a[72:75], v[148:151], 0\n\t"
    "ds_read_b64_tr_b16 v[162:163], %[hb0] offset:4608\n\t"
    "v_mfma_f32_16x16x32_bf16 v[8:11], a[72:75], v[152:155], 0\n\t"
    "buffer_load_dwordx4 a[48:51], %[voff], s[36:39], s56 offen offset:0 sc1\n\t"
    "v_mfma_f32_16x16x32_bf16 v[12:15], a[72:75], v[156:159], 0\n\t"
    "s_waitcnt vmcnt(35)\n\t"
    "v_mfma_f32_16x16x32_bf16 v[16:19], a[76:79], v[144:147], 0\n\t"
    "ds_read_b64_tr_b16 v[164:165], %[hb1] offset:4096\n\t"
    "v_mfma_f32_16x16x32_bf16 v[20:23], a[76:79], v[148:151], 0\n\t"
    "ds_read_b64_tr_b16 v[166:167], %[hb1] offset:4608\n\t"
    "v_mfma_f32_16x16x32_bf16 v[24:27], a[76:79], v[152:155], 0\n\t"
    "buffer_load_dwordx4 a[52:55], %[voff], s[36:39], s56 offen offset:1024 sc1\n\t"
    "v_mfma_f32_16x16x32_bf16 v[28:31], a[76:79], v[156:159], 0\n\t"
    "s_waitcnt vmcnt(35)\n\t"
    "v_mfma_f32_16x16x32_bf16 v[32:35], a[80:83], v[144:147], 0\n\t"
    "ds_read_b64_tr_b16 v[168:169], %[hb2] offset:4096\n\t"
    "v_mfma_f32_16x16x32_bf16 v[36:39], a[80:83], v[148:151], 0\n\t"
    "ds_read_b64_tr_b16 v[170:171], %[hb2] offset:4608\n\t"
    "v_mfma_f32_16x16x32_bf16 v[40:43], a[80:83], v[152:155], 0\n\t"
    "buffer_load_dwordx4 a[56:59], %[voff], s[36:39], s56 offen offset:2048 sc1\n\t"
    "v_mfma_f32_16x16x32_bf16 v[44:47], a[80:83], v[156:159], 0\n\t"
    "s_waitcnt vmcnt(35)\n\t"
    "v_mfma_f32_16x16x32_bf16 v[48:51], a[84:87], v[144:147], 0\n\t"
    "ds_read_b64_tr_b16 v[172:173], %[hb3] offset:4096\n\t"
    "v_mfma_f32_16x16x32_bf16 v[52:55], a[84:87], v[148:151], 0\n\t"
    "ds_read_b64_tr_b16 v[174:175], %[hb3] offset:4608\n\t"
    "v_mfma_f32_16x16x32_bf16 v[56:59], a[84:87], v[152:155], 0\n\t"
    "buffer_load_dwordx4 a[60:63], %[voff], s[36:39], s56 offen offset:3072 sc1\n\t"
    "v_mfma_f32_16x16x32_bf16 v[60:63], a[84:87], v[156:159], 0\n\t"
    "s_add_u32 s56, s56, 4096\n\t"
    "s_waitcnt vmcnt(35)\n\t"
    "v_mfma_f32_16x16x32_bf16 v[64:67], a[88:91], v[144:147], 0\n\t"
    "v_mfma_f32_16x16x32_bf16 v[68:71], a[88:91], v[148:151], 0\n\t"
    "v_mfma_f32_16x16x32_bf16 v[72:75], a[88:91], v[152:155], 0\n\t"
    "buffer_load_dwordx4 a[64:67], %[voff], s[36:39], s56 offen offset:0 sc1\n\t"
    "v_mfma_f32_16x16x32_bf16 v[76:79], a[88:91], v[156:159], 0\n\t"
    "s_waitcnt vmcnt(35)\n\t"
    "v_mfma_f32_16x16x32_bf16 v[80:83], a[92:95], v[144:147], 0\n\t"
    "v_mfma_f32_16x16x32_bf16 v[84:87], a[92:95], v[148:151], 0\n\t"
    "v_mfma_f32_16x16x32_bf16 v[88:91], a[92:95], v[152:155], 0\n\t"
    "buffer_load_dwordx4 a[68:71], %[voff], s[36:39], s56 offen offset:1024 sc1\n\t"
    "v_mfma_f32_16x16x32_bf16 v[92:95], a[92:95], v[156:159], 0\n\t"
    "s_waitcnt vmcnt(35)\n\t"
    "v_mfma_f32_16x16x32_bf16 v[96:99], a[96:99], v[144:147], 0\n\t"
    "v_mfma_f32_16x16x32_bf16 v[100:103], a[96:99], v[148:151], 0\n\t"
    "v_mfma_f32_16x16x32_bf16 v[104:107], a[96:99], v[152:155], 0\n\t"
    "buffer_load_dwordx4 a[72:75], %[voff], s[36:39], s56 offen offset:2048 sc1\n\t"
    "v_mfma_f32_16x16x32_bf16 v[108:111], a[96:99], v[156:159], 0\n\t"
    "s_waitcnt vmcnt(35)\n\t"
    "v_mfma_f32_16x16x32_bf16 v[112:115], a[100:103], v[144:147], 0\n\t"
    "v_mfma_f32_16x16x32_bf16 v[116:119], a[100:103], v[148:151], 0\n\t"
    "v_mfma_f32_16x16x32_bf16 v[120:123], a[100:103], v[152:155], 0\n\t"
    "v_mfma_f32_16x16x32_bf16 v[124:127], a[100:103], v[156:159], 0\n\t"
    "s_waitcnt vmcnt(34)\n\t"
    "v_mfma_f32_16x16x32_bf16 v[128:131], a[104:107], v[144:147], 0\n\t"
    "v_mfma_f32_16x16x32_bf16 v[132:135], a[104:107], v[148:151], 0\n\t"
    "v_mfma_f32_16x16x32_bf16 v[136:139], a[104:107], v[152:155], 0\n\t"
    "buffer_load_dwordx4 a[76:79], %[voff], s[36:39], s56 offen offset:3072 sc1\n\t"
    "v_mfma_f32_16x16x32_bf16 v[140:143], a[104:107], v[156:159], 0\n\t"
    "s_add_u32 s56, s56, 4096\n\t"
    "s_waitcnt lgkmcnt(0)\n\t"
    "v_mfma_f32_16x16x32_bf16 v[192:195], v[200:203], v[160:163], v[192:195]\n\t"
    "v_mfma_f32_16x16x32_bf16 v[196:199], v[160:163], v[160:163], v[196:199]\n\t"
    "s_waitcnt vmcnt(34)\n\t"
    "v_mfma_f32_16x16x32_bf16 v[0:3], a[108:111], v[160:163], v[0:3]\n\t"
    "ds_read_b64_tr_b16 v[176:177], %[hb0] offset:8192\n\t"
    "v_mfma_f32_16x16x32_bf16 v[4:7], a[108:111], v[164:167], v[4:7]\n\t"
    "ds_read_b64_tr_b16 v[178:179], %[hb0] offset:8704\n\t"
    "v_mfma_f32_16x16x32_bf16 v[8:11], a[108:111], v[168:171], v[8:11]\n\t"
    "buffer_load_dwordx4 a[80:83], %[voff], s[36:39], s56 offen offset:0 sc1\n\t"
    "v_mfma_f32_16x16x32_bf16 v[12:15], a[108:111], v[172:175], v[12:15]\n\t"
    "s_waitcnt vmcnt(34)\n\t"
    "v_mfma_f32_16x16x32_bf16 v[16:19], a[112:115], v[160:163], v[16:19]\n\t"
    "ds_read_b64_tr_b16 v[180:181], %[hb1] offset:8192\n\t"
    "v_mfma_f32_16x16x32_bf16 v[20:23], a[112:115], v[164:167], v[20:23]\n\t"
    "ds_read_b64_tr_b16 v[182:183], %[hb1] offset:8704\n\t"
    "v_mfma_f32_16x16x32_bf16 v[24:27], a[112:115], v[168:171], v[24:27]\n\t"
    "buffer_load_dwordx4 a[84:87], %[voff], s[36:39], s56 offen offset:1024 sc1\n\t"
    "v_mfma_f32_16x16x32_bf16 v[28:31], a[112:115], v[172:175], v[28:31]\n\t"
    "s_waitcnt vmcnt(34)\n\t"
    "v_mfma_f32_16x16x32_bf16 v[32:35], a[116:119], v[160:163], v[32:35]\n\t"
    "ds_read_b64_tr_b16 v[184:185], %[hb2] offset:8192\n\t"
    "v_mfma_f32_16x16x32_bf16 v[36:39], a[116:119], v[164:167], v[36:39]\n\t"
    "ds_read_b64_tr_b16 v[186:187], %[hb2] offset:8704\n\t"
    "v_mfma_f32_16x16x32_bf16 v[40:43], a[116:119], v[168:171], v[40:43]\n\t"
    "buffer_load_dwordx4 a[88:91], %[voff], s[36:39], s56 offen offset:2048 sc1\n\t"
    "v_mfma_f32_16x16x32_bf16 v[44:47], a[116:119], v[172:175], v[44:47]\n\t"
    "s_waitcnt vmcnt(34)\n\t"
    "v_mfma_f32_16x16x32_bf16 v[48:51], a[120:123], v[160:163], v[48:51]\n\t"
    "ds_read_b64_tr_b16 v[188:189], %[hb3] offset:8192\n\t"
    "v_mfma_f32_16x16x32_bf16 v[52:55], a[120:123], v[164:167], v[52:55]\n\t"
    "ds_read_b64_tr_b16 v[190:191], %[hb3] offset:8704\n\t"
    "v_mfma_f32_16x16x32_bf16 v[56:59], a[120:123], v[168:171], v[56:59]\n\t"
    "buffer_load_dwordx4 a[92:95], %[voff], s[36:39], s56 offen offset:3072 sc1\n\t"
    "v_mfma_f32_16x16x32_bf16 v[60:63], a[120:123], v[172:175], v[60:63]\n\t"
    "s_add_u32 s56, s56, 4096\n\t"
    "s_waitcnt vmcnt(34)\n\t"
    "v_mfma_f32_16x16x32_bf16 v[64:67], a[124:127], v[160:163], v[64:67]\n\t"
    "v_mfma_f32_16x16x32_bf16 v[68:71], a[124:127], v[164:167], v[68:71]\n\t"
    "s_waitcnt vmcnt(14)\n\t"
    "ds_write_b128 %[hwr], a[144:147] offset:0\n\t"
    "v_mfma_f32_16x16x32_bf16 v[72:75], a[124:127], v[168:171], v[72:75]\n\t"
    "buffer_load_dwordx4 a[144:147], %[voff], s[40:43], s57 offen sc1\n\t"
    "s_add_u32 s57, s57, 0x1000\n\t"
    "v_mfma_f32_16x16x32_bf16 v[76:79], a[124:127], v[172:175], v[76:79]\n\t"
    "buffer_load_dwordx4 a[96:99], %[voff], s[36:39], s56 offen offset:0 sc1\n\t"
    "s_waitcnt vmcnt(35)\n\t"
    "v_mfma_f32_16x16x32_bf16 v[80:83], a[128:131], v[160:163], v[80:83]\n\t"
    "v_mfma_f32_16x16x32_bf16 v[84:87], a[128:131], v[164:167], v[84:87]\n\t"
    "s_waitcnt vmcnt(15)\n\t"
    "ds_write_b128 %[hwr], a[148:151] offset:4096\n\t"
    "v_mfma_f32_16x16x32_bf16 v[88:91], a[128:131], v[168:171], v[88:91]\n\t"
    "buffer_load_dwordx4 a[148:151], %[voff], s[40:43], s57 offen sc1\n\t"
    "s_add_u32 s57, s57, 0x1000\n\t"
    "v_mfma_f32_16x16x32_bf16 v[92:95], a[128:131], v[172:175], v[92:95]\n\t"
    "buffer_load_dwordx4 a[100:103], %[voff], s[36:39], s56 offen offset:1024 sc1\n\t"
    "s_waitcnt vmcnt(36)\n\t"
    "v_mfma_f32_16x16x32_bf16 v[96:99], a[132:135], v[160:163], v[96:99]\n\t"
    "v_mfma_f32_16x16x32_bf16 v[100:103], a[132:135], v[164:167], v[100:103]\n\t"
    "s_waitcnt vmcnt(16)\n\t"
    "ds_write_b128 %[hwr], a[152:155] offset:8192\n\t"
    "v_mfma_f32_16x16x32_bf16 v[104:107], a[132:135], v[168:171], v[104:107]\n\t"
    "buffer_load_dwordx4 a[152:155], %[voff], s[40:43], s57 offen sc1\n\t"
    "s_add_u32 s57, s57, 0x1000\n\t"
    "v_mfma_f32_16x16x32_bf16 v[108:111], a[132:135], v[172:175], v[108:111]\n\t"
    "s_waitcnt vmcnt(36)\n\t"
    "v_mfma_f32_16x16x32_bf16 v[112:115], a[136:139], v[160:163], v[112:115]\n\t"
    "v_mfma_f32_16x16x32_bf16 v[116:119], a[136:139], v[164:167], v[116:119]\n\t"
    "v_mfma_f32_16x16x32_bf16 v[120:123], a[136:139], v[168:171], v[120:123]\n\t"
    "buffer_load_dwordx4 a[104:107], %[voff], s[36:39], s56 offen offset:2048 sc1\n\t"
    "v_mfma_f32_16x16x32_bf16 v[124:127], a[136:139], v[172:175], v[124:127]\n\t"
    "s_waitcnt vmcnt(36)\n\t"
    "v_mfma_f32_16x16x32_bf16 v[128:131], a[140:143], v[160:163], v[128:131]\n\t"
    "v_mfma_f32_16x16x32_bf16 v[132:135], a[140:143], v[164:167], v[132:135]\n\t"
    "v_add_u32 %[hwr], 0x3000, %[hwr]\n\t"
    "v_mfma_f32_16x16x32_bf16 v[136:139], a[140:143], v[168:171], v[136:139]\n\t"
    "buffer_load_dwordx4 a[108:111], %[voff], s[36:39], s56 offen offset:3072 sc1\n\t"
    "v_mfma_f32_16x16x32_bf16 v[140:143], a[140:143], v[172:175], v[140:143]\n\t"
    "s_add_u32 s56, s56, 4096\n\t"
    "s_waitcnt lgkmcnt(0)\n\t"
    "s_barrier\n\t"
    "v_mfma_f32_16x16x32_bf16 v[192:195], v[200:203], v[176:179], v[192:195]\n\t"
    "v_mfma_f32_16x16x32_bf16 v[196:199], v[176:179], v[176:179], v[196:199]\n\t"
    "s_waitcnt vmcnt(36)\n\t"
    "v_mfma_f32_16x16x32_bf16 v[0:3], a[0:3], v[176:179], v[0:3]\n\t"
    "ds_read_b64_tr_b16 v[144:145], %[hb0] offset:12288\n\t"
    "v_mfma_f32_16x16x32_bf16 v[4:7], a[0:3], v[180:183], v[4:7]\n\t"
    "ds_read_b64_tr_b16 v[146:147], %[hb0] offset:12800\n\t"
    "v_mfma_f32_16x16x32_bf16 v[8:11], a[0:3], v[184:187], v[8:11]\n\t"
    "buffer_load_dwordx4 a[112:115], %[voff], s[36:39], s56 offen offset:0 sc1\n\t"
    "v_mfma_f32_16x16x32_bf16 v[12:15], a[0:3], v[188:191], v[12:15]\n\t"
    "s_waitcnt vmcnt(36)\n\t"
    "v_mfma_f32_16x16x32_bf16 v[16:19], a[4:7], v[176:179], v[16:19]\n\t"
    "ds_read_b64_tr_b16 v[148:149], %[hb1] offset:12288\n\t"
    "v_mfma_f32_16x16x32_bf16 v[20:23], a[4:7], v[180:183], v[20:23]\n\t"
    "ds_read_b64_tr_b16 v[150:151], %[hb1] offset:12800\n\t"
    "v_mfma_f32_16x16x32_bf16 v[24:27], a[4:7], v[184:187], v[24:27]\n\t"
    "buffer_load_dwordx4 a[116:119], %[voff], s[36:39], s56 offen offset:1024 sc1\n\t"
    "v_mfma_f32_16x16x32_bf16 v[28:31], a[4:7], v[188:191], v[28:31]\n\t"
    "s_waitcnt vmcnt(36)\n\t"
    "v_mfma_f32_16x16x32_bf16 v[32:35], a[8:11], v[176:179], v[32:35]\n\t"
    "ds_read_b64_tr_b16 v[152:153], %[hb2] offset:12288\n\t"
    "v_mfma_f32_16x16x32_bf16 v[36:39], a[8:11], v[180:183], v[36:39]\n\t"
    "ds_read_b64_tr_b16 v[154:155], %[hb2] offset:12800\n\t"
    "v_mfma_f32_16x16x32_bf16 v[40:43], a[8:11], v[184:187], v[40:43]\n\t"
    "buffer_load_dwordx4 a[120:123], %[voff], s[36:39], s56 offen offset:2048 sc1\n\t"
    "v_mfma_f32_16x16x32_bf16 v[44:47], a[8:11], v[188:191], v[44:47]\n\t"
    "s_waitcnt vmcnt(36)\n\t"
    "v_mfma_f32_16x16x32_bf16 v[48:51], a[12:15], v[176:179], v[48:51]\n\t"
    "ds_read_b64_tr_b16 v[156:157], %[hb3] offset:12288\n\t"
    "v_mfma_f32_16x16x32_bf16 v[52:55], a[12:15], v[180:183], v[52:55]\n\t"
    "ds_read_b64_tr_b16 v[158:159], %[hb3] offset:12800\n\t"
    "v_mfma_f32_16x16x32_bf16 v[56:59], a[12:15], v[184:187], v[56:59]\n\t"
    "buffer_load_dwordx4 a[124:127], %[voff], s[36:39], s56 offen offset:3072 sc1\n\t"
    "v_mfma_f32_16x16x32_bf16 v[60:63], a[12:15], v[188:191], v[60:63]\n\t"
    "s_add_u32 s56, s56, 4096\n\t"
    "s_waitcnt vmcnt(36)\n\t"
    "v_mfma_f32_16x16x32_bf16 v[64:67], a[16:19], v[176:179], v[64:67]\n\t"
    "v_mfma_f32_16x16x32_bf16 v[68:71], a[16:19], v[180:183], v[68:71]\n\t"
    "v_add_u32 %[hb0], 0x3000, %[hb0]\n\t"
    "v_mfma_f32_16x16x32_bf16 v[72:75], a[16:19], v[184:187], v[72:75]\n\t"
    "buffer_load_dwordx4 a[128:131], %[voff], s[36:39], s56 offen offset:0 sc1\n\t"
    "v_mfma_f32_16x16x32_bf16 v[76:79], a[16:19], v[188:191], v[76:79]\n\t"
    "s_waitcnt vmcnt(36)\n\t"
    "v_mfma_f32_16x16x32_bf16 v[80:83], a[20:23], v[176:179], v[80:83]\n\t"
    "v_mfma_f32_16x16x32_bf16 v[84:87], a[20:23], v[180:183], v[84:87]\n\t"
    "v_add_u32 %[hb1], 0x3000, %[hb1]\n\t"
    "v_mfma_f32_16x16x32_bf16 v[88:91], a[20:23], v[184:187], v[88:91]\n\t"
    "v_mfma_f32_16x16x32_bf16 v[92:95], a[20:23], v[188:191], v[92:95]\n\t"
    "s_waitcnt vmcnt(35)\n\t"
    "v_mfma_f32_16x16x32_bf16 v[96:99], a[24:27], v[176:179], v[96:99]\n\t"
    "v_mfma_f32_16x16x32_bf16 v[100:103], a[24:27], v[180:183], v[100:103]\n\t"
    "v_add_u32 %[hb2], 0x3000, %[hb2]\n\t"
    "v_mfma_f32_16x16x32_bf16 v[104:107], a[24:27], v[184:187], v[104:107]\n\t"
    "buffer_load_dwordx4 a[132:135], %[voff], s[36:39], s56 offen offset:1024 sc1\n\t"
    "v_mfma_f32_16x16x32_bf16 v[108:111], a[24:27], v[188:191], v[108:111]\n\t"
    "s_waitcnt vmcnt(35)\n\t"
    "v_mfma_f32_16x16x32_bf16 v[112:115], a[28:31], v[176:179], v[112:115]\n\t"
    "v_mfma_f32_16x16x32_bf16 v[116:119], a[28:31], v[180:183], v[116:119]\n\t"
    "v_add_u32 %[hb3], 0x3000, %[hb3]\n\t"
    "v_mfma_f32_16x16x32_bf16 v[120:123], a[28:31], v[184:187], v[120:123]\n\t"
    "buffer_load_dwordx4 a[136:139], %[voff], s[36:39], s56 offen offset:2048 sc1\n\t"
    "v_mfma_f32_16x16x32_bf16 v[124:127], a[28:31], v[188:191], v[124:127]\n\t"
    "s_waitcnt vmcnt(35)\n\t"
    "v_mfma_f32_16x16x32_bf16 v[128:131], a[32:35], v[176:179], v[128:131]\n\t"
    "v_mfma_f32_16x16x32_bf16 v[132:135], a[32:35], v[180:183], v[132:135]\n\t"
    "v_mfma_f32_16x16x32_bf16 v[136:139], a[32:35], v[184:187], v[136:139]\n\t"
    "buffer_load_dwordx4 a[140:143], %[voff], s[36:39], s56 offen offset:3072 sc1\n\t"
    "v_mfma_f32_16x16x32_bf16 v[140:143], a[32:35], v[188:191], v[140:143]\n\t"
    "s_add_u32 s56, s56, 4096\n\t"
    "s_waitcnt lgkmcnt(0)\n\t"
    "v_mfma_f32_16x16x32_bf16 v[192:195], v[200:203], v[144:147], v[192:195]\n\t"
    "v_mfma_f32_16x16x32_bf16 v[196:199], v[144:147], v[144:147], v[196:199]\n\t"
    "s_waitcnt vmcnt(35)\n\t"
    "v_mfma_f32_16x16x32_bf16 v[0:3], a[36:39], v[144:147], v[0:3]\n\t"
    "ds_read_b64_tr_b16 v[160:161], %[hb0] offset:4096\n\t"
    "v_mfma_f32_16x16x32_bf16 v[4:7], a[36:39], v[148:151], v[4:7]\n\t"
    "ds_read_b64_tr_b16 v[162:163], %[hb0] offset:4608\n\t"
    "v_mfma_f32_16x16x32_bf16 v[8:11], a[36:39], v[152:155], v[8:11]\n\t"
    "buffer_load_dwordx4 a[0:3], %[voff], s[36:39], s56 offen offset:0 sc1\n\t"
    "v_mfma_f32_16x16x32_bf16 v[12:15], a[36:39], v[156:159], v[12:15]\n\t"
    "s_waitcnt vmcnt(35)\n\t"
    "v_mfma_f32_16x16x32_bf16 v[16:19], a[40:43], v[144:147], v[16:19]\n\t"
    "ds_read_b64_tr_b16 v[164:165], %[hb1] offset:4096\n\t"
    "v_mfma_f32_16x16x32_bf16 v[20:23], a[40:43], v[148:151], v[20:23]\n\t"
    "ds_read_b64_tr_b16 v[166:167], %[hb1] offset:4608\n\t"
    "v_mfma_f32_16x16x32_bf16 v[24:27], a[40:43], v[152:155], v[24:27]\n\t"
    "buffer_load_dwordx4 a[4:7], %[voff], s[36:39], s56 offen offset:1024 sc1\n\t"
    "v_mfma_f32_16x16x32_bf16 v[28:31], a[40:43], v[156:159], v[28:31]\n\t"
    "s_waitcnt vmcnt(35)\n\t"
    "v_mfma_f32_16x16x32_bf16 v[32:35], a[44:47], v[144:147], v[32:35]\n\t"
    "ds_read_b64_tr_b16 v[168:169], %[hb2] offset:4096\n\t"
    "v_mfma_f32_16x16x32_bf16 v[36:39], a[44:47], v[148:151], v[36:39]\n\t"
    "ds_read_b64_tr_b16 v[170:171], %[hb2] offset:4608\n\t"
    "v_mfma_f32_16x16x32_bf16 v[40:43], a[44:47], v[152:155], v[40:43]\n\t"
    "buffer_load_dwordx4 a[8:11], %[voff], s[36:39], s56 offen offset:2048 sc1\n\t"
    "v_mfma_f32_16x16x32_bf16 v[44:47], a[44:47], v[156:159], v[44:47]\n\t"
    "s_waitcnt vmcnt(29)\n\t"
    "v_mfma_f32_16x16x32_bf16 v[48:51], a[48:51], v[144:147], v[48:51]\n\t"
    "ds_read_b64_tr_b16 v[172:173], %[hb3] offset:4096\n\t"
    "v_mfma_f32_16x16x32_bf16 v[52:55], a[48:51], v[148:151], v[52:55]\n\t"
    "ds_read_b64_tr_b16 v[174:175], %[hb3] offset:4608\n\t"
    "v_mfma_f32_16x16x32_bf16 v[56:59], a[48:51], v[152:155], v[56:59]\n\t"
    "v_mfma_f32_16x16x32_bf16 v[60:63], a[48:51], v[156:159], v[60:63]\n\t"
    "s_waitcnt vmcnt(28)\n\t"
    "v_mfma_f32_16x16x32_bf16 v[64:67], a[52:55], v[144:147], v[64:67]\n\t"
    "v_mfma_f32_16x16x32_bf16 v[68:71], a[52:55], v[148:151], v[68:71]\n\t"
    "v_mfma_f32_16x16x32_bf16 v[72:75], a[52:55], v[152:155], v[72:75]\n\t"
    "buffer_load_dwordx4 a[12:15], %[voff], s[36:39], s56 offen offset:3072 sc1\n\t"
    "v_mfma_f32_16x16x32_bf16 v[76:79], a[52:55], v[156:159], v[76:79]\n\t"
    "s_add_u32 s56, s56, 4096\n\t"
    "s_waitcnt vmcnt(28)\n\t"
    "v_mfma_f32_16x16x32_bf16 v[80:83], a[56:59], v[144:147], v[80:83]\n\t"
    "v_mfma_f32_16x16x32_bf16 v[84:87], a[56:59], v[148:151], v[84:87]\n\t"
    "v_mfma_f32_16x16x32_bf16 v[88:91], a[56:59], v[152:155], v[88:91]\n\t"
    "buffer_load_dwordx4 a[16:19], %[voff], s[36:39], s56 offen offset:0 sc1\n\t"
    "v_mfma_f32_16x16x32_bf16 v[92:95], a[56:59], v[156:159], v[92:95]\n\t"
    "s_waitcnt vmcnt(28)\n\t"
    "v_mfma_f32_16x16x32_bf16 v[96:99], a[60:63], v[144:147], v[96:99]\n\t"
    "v_mfma_f32_16x16x32_bf16 v[100:103], a[60:63], v[148:151], v[100:103]\n\t"
    "v_mfma_f32_16x16x32_bf16 v[104:107], a[60:63], v[152:155], v[104:107]\n\t"
    "buffer_load_dwordx4 a[20:23], %[voff], s[36:39], s56 offen offset:1024 sc1\n\t"
    "v_mfma_f32_16x16x32_bf16 v[108:111], a[60:63], v[156:159], v[108:111]\n\t"
    "s_waitcnt vmcnt(28)\n\t"
    "v_mfma_f32_16x16x32_bf16 v[112:115], a[64:67], v[144:147], v[112:115]\n\t"
    "v_mfma_f32_16x16x32_bf16 v[116:119], a[64:67], v[148:151], v[116:119]\n\t"
    "v_mfma_f32_16x16x32_bf16 v[120:123], a[64:67], v[152:155], v[120:123]\n\t"
    "buffer_load_dwordx4 a[24:27], %[voff], s[36:39], s56 offen offset:2048 sc1\n\t"
    "v_mfma_f32_16x16x32_bf16 v[124:127], a[64:67], v[156:159], v[124:127]\n\t"
    "s_waitcnt vmcnt(28)\n\t"
    "v_mfma_f32_16x16x32_bf16 v[128:131], a[68:71], v[144:147], v[128:131]\n\t"
    "v_mfma_f32_16x16x32_bf16 v[132:135], a[68:71], v[148:151], v[132:135]\n\t"
    "v_mfma_f32_16x16x32_bf16 v[136:139], a[68:71], v[152:155], v[136:139]\n\t"
    "buffer_load_dwordx4 a[28:31], %[voff], s[36:39], s56 offen offset:3072 sc1\n\t"
    "v_mfma_f32_16x16x32_bf16 v[140:143], a[68:71], v[156:159], v[140:143]\n\t"
    "s_add_u32 s56, s56, 4096\n\t"
    "s_waitcnt lgkmcnt(0)\n\t"
    "v_mfma_f32_16x16x32_bf16 v[192:195], v[200:203], v[160:163], v[192:195]\n\t"
    "v_mfma_f32_16x16x32_bf16 v[196:199], v[160:163], v[160:163], v[196:199]\n\t"
    "s_waitcnt vmcnt(28)\n\t"
    "v_mfma_f32_16x16x32_bf16 v[0:3], a[72:75], v[160:163], v[0:3]\n\t"
    "ds_read_b64_tr_b16 v[176:177], %[hb0] offset:8192\n\t"
    "v_mfma_f32_16x16x32_bf16 v[4:7], a[72:75], v[164:167], v[4:7]\n\t"
    "ds_read_b64_tr_b16 v[178:179], %[hb0] offset:8704\n\t"
    "v_mfma_f32_16x16x32_bf16 v[8:11], a[72:75], v[168:171], v[8:11]\n\t"
    "buffer_load_dwordx4 a[32:35], %[voff], s[36:39], s56 offen offset:0 sc1\n\t"
    "v_mfma_f32_16x16x32_bf16 v[12:15], a[72:75], v[172:175], v[12:15]\n\t"
    "s_waitcnt vmcnt(28)\n\t"
    "v_mfma_f32_16x16x32_bf16 v[16:19], a[76:79], v[160:163], v[16:19]\n\t"
    "ds_read_b64_tr_b16 v[180:181], %[hb1] offset:8192\n\t"
    "v_mfma_f32_16x16x32_bf16 v[20:23], a[76:79], v[164:167], v[20:23]\n\t"
    "ds_read_b64_tr_b16 v[182:183], %[hb1] offset:8704\n\t"
    "v_mfma_f32_16x16x32_bf16 v[24:27], a[76:79], v[168:171], v[24:27]\n\t"
    "buffer_load_dwordx4 a[36:39], %[voff], s[36:39], s56 offen offset:1024 sc1\n\t"
    "v_mfma_f32_16x16x32_bf16 v[28:31], a[76:79], v[172:175], v[28:31]\n\t"
    "s_waitcnt vmcnt(28)\n\t"
    "v_mfma_f32_16x16x32_bf16 v[32:35], a[80:83], v[160:163], v[32:35]\n\t"
    "ds_read_b64_tr_b16 v[184:185], %[hb2] offset:8192\n\t"
    "v_mfma_f32_16x16x32_bf16 v[36:39], a[80:83], v[164:167], v[36:39]\n\t"
    "ds_read_b64_tr_b16 v[186:187], %[hb2] offset:8704\n\t"
    "v_mfma_f32_16x16x32_bf16 v[40:43], a[80:83], v[168:171], v[40:43]\n\t"
    "v_mfma_f32_16x16x32_bf16 v[44:47], a[80:83], v[172:175], v[44:47]\n\t"
    "s_waitcnt vmcnt(27)\n\t"
    "v_mfma_f32_16x16x32_bf16 v[48:51], a[84:87], v[160:163], v[48:51]\n\t"
    "ds_read_b64_tr_b16 v[188:189], %[hb3] offset:8192\n\t"
    "v_mfma_f32_16x16x32_bf16 v[52:55], a[84:87], v[164:167], v[52:55]\n\t"
    "ds_read_b64_tr_b16 v[190:191], %[hb3] offset:8704\n\t"
    "v_mfma_f32_16x16x32_bf16 v[56:59], a[84:87], v[168:171], v[56:59]\n\t"
    "buffer_load_dwordx4 a[40:43], %[voff], s[36:39], s56 offen offset:2048 sc1\n\t"
    "v_mfma_f32_16x16x32_bf16 v[60:63], a[84:87], v[172:175], v[60:63]\n\t"
    "s_waitcnt vmcnt(27)\n\t"
    "v_mfma_f32_16x16x32_bf16 v[64:67], a[88:91], v[160:163], v[64:67]\n\t"
    "v_mfma_f32_16x16x32_bf16 v[68:71], a[88:91], v[164:167], v[68:71]\n\t"
    "s_waitcnt vmcnt(25)\n\t"
    "ds_write_b128 %[hwr], a[144:147] offset:0\n\t"
    "v_mfma_f32_16x16x32_bf16 v[72:75], a[88:91], v[168:171], v[72:75]\n\t"
    "buffer_load_dwordx4 a[144:147], %[voff], s[40:43], s57 offen sc1\n\t"
    "s_add_u32 s57, s57, 0x1000\n\t"
    "v_mfma_f32_16x16x32_bf16 v[76:79], a[88:91], v[172:175], v[76:79]\n\t"
    "buffer_load_dwordx4 a[44:47], %[voff], s[36:39], s56 offen offset:3072 sc1\n\t"
    "s_waitcnt vmcnt(28)\n\t"
    "v_mfma_f32_16x16x32_bf16 v[80:83], a[92:95], v[160:163], v[80:83]\n\t"
    "s_add_u32 s56, s56, 4096\n\t"
    "v_mfma_f32_16x16x32_bf16 v[84:87], a[92:95], v[164:167], v[84:87]\n\t"
    "s_waitcnt vmcnt(25)\n\t"
    "ds_write_b128 %[hwr], a[148:151] offset:4096\n\t"
    "v_mfma_f32_16x16x32_bf16 v[88:91], a[92:95], v[168:171], v[88:91]\n\t"
    "buffer_load_dwordx4 a[148:151], %[voff], s[40:43], s57 offen sc1\n\t"
    "s_add_u32 s57, s57, 0x1000\n\t"
    "v_mfma_f32_16x16x32_bf16 v[92:95], a[92:95], v[172:175], v[92:95]\n\t"
    "buffer_load_dwordx4 a[48:51], %[voff], s[36:39], s56 offen offset:0 sc1\n\t"
    "s_waitcnt vmcnt(28)\n\t"
    "v_mfma_f32_16x16x32_bf16 v[96:99], a[96:99], v[160:163], v[96:99]\n\t"
    "v_mfma_f32_16x16x32_bf16 v[100:103], a[96:99], v[164:167], v[100:103]\n\t"
    "s_waitcnt vmcnt(25)\n\t"
    "ds_write_b128 %[hwr], a[152:155] offset:8192\n\t"
    "v_mfma_f32_16x16x32_bf16 v[104:107], a[96:99], v[168:171], v[104:107]\n\t"
    "buffer_load_dwordx4 a[152:155], %[voff], s[40:43], s57 offen sc1\n\t"
    "s_add_u32 s57, s57, 0x1000\n\t"
    "v_mfma_f32_16x16x32_bf16 v[108:111], a[96:99], v[172:175], v[108:111]\n\t"
    "buffer_load_dwordx4 a[52:55], %[voff], s[36:39], s56 offen offset:1024 sc1\n\t"
    "s_waitcnt vmcnt(28)\n\t"
    "v_mfma_f32_16x16x32_bf16 v[112:115], a[100:103], v[160:163], v[112:115]\n\t"
    "v_mfma_f32_16x16x32_bf16 v[116:119], a[100:103], v[164:167], v[116:119]\n\t"
    "v_mfma_f32_16x16x32_bf16 v[120:123], a[100:103], v[168:171], v[120:123]\n\t"
    "buffer_load_dwordx4 a[56:59], %[voff], s[36:39], s56 offen offset:2048 sc1\n\t"
    "v_mfma_f32_16x16x32_bf16 v[124:127], a[100:103], v[172:175], v[124:127]\n\t"
    "s_waitcnt vmcnt(27)\n\t"
    "v_mfma_f32_16x16x32_bf16 v[128:131], a[104:107], v[160:163], v[128:131]\n\t"
    "v_mfma_f32_16x16x32_bf16 v[132:135], a[104:107], v[164:167], v[132:135]\n\t"
    "v_add_u32 %[hwr], 0x3000, %[hwr]\n\t"
    "v_mfma_f32_16x16x32_bf16 v[136:139], a[104:107], v[168:171], v[136:139]\n\t"
    "buffer_load_dwordx4 a[60:63], %[voff], s[36:39], s56 offen offset:3072 sc1\n\t"
    "v_mfma_f32_16x16x32_bf16 v[140:143], a[104:107], v[172:175], v[140:143]\n\t"
    "s_add_u32 s56, s56, 4096\n\t"
    "s_waitcnt lgkmcnt(0)\n\t"
    "s_barrier\n\t"
    "v_mfma_f32_16x16x32_bf16 v[192:195], v[200:203], v[176:179], v[192:195]\n\t"
    "v_mfma_f32_16x16x32_bf16 v[196:199], v[176:179], v[176:179], v[196:199]\n\t"
    "s_waitcnt vmcnt(27)\n\t"
    "v_mfma_f32_16x16x32_bf16 v[0:3], a[108:111], v[176:179], v[0:3]\n\t"
    "ds_read_b64_tr_b16 v[144:145], %[hb0] offset:12288\n\t"
    "v_mfma_f32_16x16x32_bf16 v[4:7], a[108:111], v[180:183], v[4:7]\n\t"
    "ds_read_b64_tr_b16 v[146:147], %[hb0] offset:12800\n\t"
    "v_mfma_f32_16x16x32_bf16 v[8:11], a[108:111], v[184:187], v[8:11]\n\t"
    "buffer_load_dwordx4 a[64:67], %[voff], s[36:39], s56 offen offset:0 sc1\n\t"
    "v_mfma_f32_16x16x32_bf16 v[12:15], a[108:111], v[188:191], v[12:15]\n\t"
    "s_waitcnt vmcnt(27)\n\t"
    "v_mfma_f32_16x16x32_bf16 v[16:19], a[112:115], v[176:179], v[16:19]\n\t"
    "ds_read_b64_tr_b16 v[148:149], %[hb1] offset:12288\n\t"
    "v_mfma_f32_16x16x32_bf16 v[20:23], a[112:115], v[180:183], v[20:23]\n\t"
    "ds_read_b64_tr_b16 v[150:151], %[hb1] offset:12800\n\t"
    "v_mfma_f32_16x16x32_bf16 v[24:27], a[112:115], v[184:187], v[24:27]\n\t"
    "v_mfma_f32_16x16x32_bf16 v[28:31], a[112:115], v[188:191], v[28:31]\n\t"
    "s_waitcnt vmcnt(26)\n\t"
    "v_mfma_f32_16x16x32_bf16 v[32:35], a[116:119], v[176:179], v[32:35]\n\t"
    "ds_read_b64_tr_b16 v[152:153], %[hb2] offset:12288\n\t"
    "v_mfma_f32_16x16x32_bf16 v[36:39], a[116:119], v[180:183], v[36:39]\n\t"
    "ds_read_b64_tr_b16 v[154:155], %[hb2] offset:12800\n\t"
    "v_mfma_f32_16x16x32_bf16 v[40:43], a[116:119], v[184:187], v[40:43]\n\t"
    "buffer_load_dwordx4 a[68:71], %[voff], s[36:39], s56 offen offset:1024 sc1\n\t"
    "v_mfma_f32_16x16x32_bf16 v[44:47], a[116:119], v[188:191], v[44:47]\n\t"
    "s_waitcnt vmcnt(26)\n\t"
    "v_mfma_f32_16x16x32_bf16 v[48:51], a[120:123], v[176:179], v[48:51]\n\t"
    "ds_read_b64_tr_b16 v[156:157], %[hb3] offset:12288\n\t"
    "v_mfma_f32_16x16x32_bf16 v[52:55], a[120:123], v[180:183], v[52:55]\n\t"
    "ds_read_b64_tr_b16 v[158:159], %[hb3] offset:12800\n\t"
    "v_mfma_f32_16x16x32_bf16 v[56:59], a[120:123], v[184:187], v[56:59]\n\t"
    "buffer_load_dwordx4 a[72:75], %[voff], s[36:39], s56 offen offset:2048 sc1\n\t"
    "v_mfma_f32_16x16x32_bf16 v[60:63], a[120:123], v[188:191], v[60:63]\n\t"
    "s_waitcnt vmcnt(26)\n\t"
    "v_mfma_f32_16x16x32_bf16 v[64:67], a[124:127], v[176:179], v[64:67]\n\t"
    "v_mfma_f32_16x16x32_bf16 v[68:71], a[124:127], v[180:183], v[68:71]\n\t"
    "v_add_u32 %[hb0], 0x3000, %[hb0]\n\t"
    "v_mfma_f32_16x16x32_bf16 v[72:75], a[124:127], v[184:187], v[72:75]\n\t"
    "buffer_load_dwordx4 a[76:79], %[voff], s[36:39], s56 offen offset:3072 sc1\n\t"
    "v_mfma_f32_16x16x32_bf16 v[76:79], a[124:127], v[188:191], v[76:79]\n\t"
    "s_add_u32 s56, s56, 4096\n\t"
    "s_waitcnt vmcnt(26)\n\t"
    "v_mfma_f32_16x16x32_bf16 v[80:83], a[128:131], v[176:179], v[80:83]\n\t"
    "v_mfma_f32_16x16x32_bf16 v[84:87], a[128:131], v[180:183], v[84:87]\n\t"
    "v_add_u32 %[hb1], 0x3000, %[hb1]\n\t"
    "v_mfma_f32_16x16x32_bf16 v[88:91], a[128:131], v[184:187], v[88:91]\n\t"
    "buffer_load_dwordx4 a[80:83], %[voff], s[36:39], s56 offen offset:0 sc1\n\t"
    "v_mfma_f32_16x16x32_bf16 v[92:95], a[128:131], v[188:191], v[92:95]\n\t"
    "s_waitcnt vmcnt(26)\n\t"
    "v_mfma_f32_16x16x32_bf16 v[96:99], a[132:135], v[176:179], v[96:99]\n\t"
    "v_mfma_f32_16x16x32_bf16 v[100:103], a[132:135], v[180:183], v[100:103]\n\t"
    "v_add_u32 %[hb2], 0x3000, %[hb2]\n\t"
    "v_mfma_f32_16x16x32_bf16 v[104:107], a[132:135], v[184:187], v[104:107]\n\t"
    "buffer_load_dwordx4 a[84:87], %[voff], s[36:39], s56 offen offset:1024 sc1\n\t"
    "v_mfma_f32_16x16x32_bf16 v[108:111], a[132:135], v[188:191], v[108:111]\n\t"
    "s_waitcnt vmcnt(26)\n\t"
    "v_mfma_f32_16x16x32_bf16 v[112:115], a[136:139], v[176:179], v[112:115]\n\t"
    "v_mfma_f32_16x16x32_bf16 v[116:119], a[136:139], v[180:183], v[116:119]\n\t"
    "v_add_u32 %[hb3], 0x3000, %[hb3]\n\t"
    "v_mfma_f32_16x16x32_bf16 v[120:123], a[136:139], v[184:187], v[120:123]\n\t"
    "buffer_load_dwordx4 a[88:91], %[voff], s[36:39], s56 offen offset:2048 sc1\n\t"
    "v_mfma_f32_16x16x32_bf16 v[124:127], a[136:139], v[188:191], v[124:127]\n\t"
    "s_waitcnt vmcnt(26)\n\t"
    "v_mfma_f32_16x16x32_bf16 v[128:131], a[140:143], v[176:179], v[128:131]\n\t"
    "v_mfma_f32_16x16x32_bf16 v[132:135], a[140:143], v[180:183], v[132:135]\n\t"
    "v_mfma_f32_16x16x32_bf16 v[136:139], a[140:143], v[184:187], v[136:139]\n\t"
    "v_mfma_f32_16x16x32_bf16 v[140:143], a[140:143], v[188:191], v[140:143]\n\t"
    "s_waitcnt lgkmcnt(0)\n\t"
    "v_mfma_f32_16x16x32_bf16 v[192:195], v[200:203], v[144:147], v[192:195]\n\t"
    "v_mfma_f32_16x16x32_bf16 v[196:199], v[144:147], v[144:147], v[196:199]\n\t"
    "s_waitcnt vmcnt(25)\n\t"
    "v_mfma_f32_16x16x32_bf16 v[0:3], a[0:3], v[144:147], v[0:3]\n\t"
    "ds_read_b64_tr_b16 v[160:161], %[hb0] offset:4096\n\t"
    "v_mfma_f32_16x16x32_bf16 v[4:7], a[0:3], v[148:151], v[4:7]\n\t"
    "ds_read_b64_tr_b16 v[162:163], %[hb0] offset:4608\n\t"
    "v_mfma_f32_16x16x32_bf16 v[8:11], a[0:3], v[152:155], v[8:11]\n\t"
    "buffer_load_dwordx4 a[92:95], %[voff], s[36:39], s56 offen offset:3072 sc1\n\t"
    "v_mfma_f32_16x16x32_bf16 v[12:15], a[0:3], v[156:159], v[12:15]\n\t"
    "s_add_u32 s56, s56, 4096\n\t"
    "s_waitcnt vmcnt(25)\n\t"
    "v_mfma_f32_16x16x32_bf16 v[16:19], a[4:7], v[144:147], v[16:19]\n\t"
    "ds_read_b64_tr_b16 v[164:165], %[hb1] offset:4096\n\t"
    "v_mfma_f32_16x16x32_bf16 v[20:23], a[4:7], v[148:151], v[20:23]\n\t"
    "ds_read_b64_tr_b16 v[166:167], %[hb1] offset:4608\n\t"
    "v_mfma_f32_16x16x32_bf16 v[24:27], a[4:7], v[152:155], v[24:27]\n\t"
    "buffer_load_dwordx4 a[96:99], %[voff], s[36:39], s56 offen offset:0 sc1\n\t"
    "v_mfma_f32_16x16x32_bf16 v[28:31], a[4:7], v[156:159], v[28:31]\n\t"
    "s_waitcnt vmcnt(25)\n\t"
    "v_mfma_f32_16x16x32_bf16 v[32:35], a[8:11], v[144:147], v[32:35]\n\t"
    "ds_read_b64_tr_b16 v[168:169], %[hb2] offset:4096\n\t"
    "v_mfma_f32_16x16x32_bf16 v[36:39], a[8:11], v[148:151], v[36:39]\n\t"
    "ds_read_b64_tr_b16 v[170:171], %[hb2] offset:4608\n\t"
    "v_mfma_f32_16x16x32_bf16 v[40:43], a[8:11], v[152:155], v[40:43]\n\t"
    "buffer_load_dwordx4 a[100:103], %[voff], s[36:39], s56 offen offset:1024 sc1\n\t"
    "v_mfma_f32_16x16x32_bf16 v[44:47], a[8:11], v[156:159], v[44:47]\n\t"
    "s_waitcnt vmcnt(25)\n\t"
    "v_mfma_f32_16x16x32_bf16 v[48:51], a[12:15], v[144:147], v[48:51]\n\t"
    "ds_read_b64_tr_b16 v[172:173], %[hb3] offset:4096\n\t"
    "v_mfma_f32_16x16x32_bf16 v[52:55], a[12:15], v[148:151], v[52:55]\n\t"
    "ds_read_b64_tr_b16 v[174:175], %[hb3] offset:4608\n\t"
    "v_mfma_f32_16x16x32_bf16 v[56:59], a[12:15], v[152:155], v[56:59]\n\t"
    "buffer_load_dwordx4 a[104:107], %[voff], s[36:39], s56 offen offset:2048 sc1\n\t"
    "v_mfma_f32_16x16x32_bf16 v[60:63], a[12:15], v[156:159], v[60:63]\n\t"
    "s_waitcnt vmcnt(25)\n\t"
    "v_mfma_f32_16x16x32_bf16 v[64:67], a[16:19], v[144:147], v[64:67]\n\t"
    "v_mfma_f32_16x16x32_bf16 v[68:71], a[16:19], v[148:151], v[68:71]\n\t"
    "v_mfma_f32_16x16x32_bf16 v[72:75], a[16:19], v[152:155], v[72:75]\n\t"
    "buffer_load_dwordx4 a[108:111], %[voff], s[36:39], s56 offen offset:3072 sc1\n\t"
    "v_mfma_f32_16x16x32_bf16 v[76:79], a[16:19], v[156:159], v[76:79]\n\t"
    "s_add_u32 s56, s56, 4096\n\t"
    "s_waitcnt vmcnt(25)\n\t"
    "v_mfma_f32_16x16x32_bf16 v[80:83], a[20:23], v[144:147], v[80:83]\n\t"
    "v_mfma_f32_16x16x32_bf16 v[84:87], a[20:23], v[148:151], v[84:87]\n\t"
    "v_mfma_f32_16x16x32_bf16 v[88:91], a[20:23], v[152:155], v[88:91]\n\t"
    "buffer_load_dwordx4 a[112:115], %[voff], s[36:39], s56 offen offset:0 sc1\n\t"
    "v_mfma_f32_16x16x32_bf16 v[92:95], a[20:23], v[156:159], v[92:95]\n\t"
    "s_waitcnt vmcnt(25)\n\t"
    "v_mfma_f32_16x16x32_bf16 v[96:99], a[24:27], v[144:147], v[96:99]\n\t"
    "v_mfma_f32_16x16x32_bf16 v[100:103], a[24:27], v[148:151], v[100:103]\n\t"
    "v_mfma_f32_16x16x32_bf16 v[104:107], a[24:27], v[152:155], v[104:107]\n\t"
    "buffer_load_dwordx4 a[116:119], %[voff], s[36:39], s56 offen offset:1024 sc1\n\t"
    "v_mfma_f32_16x16x32_bf16 v[108:111], a[24:27], v[156:159], v[108:111]\n\t"
    "s_waitcnt vmcnt(25)\n\t"
    "v_mfma_f32_16x16x32_bf16 v[112:115], a[28:31], v[144:147], v[112:115]\n\t"
    "v_mfma_f32_16x16x32_bf16 v[116:119], a[28:31], v[148:151], v[116:119]\n\t"
    "v_mfma_f32_16x16x32_bf16 v[120:123], a[28:31], v[152:155], v[120:123]\n\t"
    "v_mfma_f32_16x16x32_bf16 v[124:127], a[28:31], v[156:159], v[124:127]\n\t"
    "s_waitcnt vmcnt(24)\n\t"
    "v_mfma_f32_16x16x32_bf16 v[128:131], a[32:35], v[144:147], v[128:131]\n\t"
    "v_mfma_f32_16x16x32_bf16 v[132:135], a[32:35], v[148:151], v[132:135]\n\t"
    "v_mfma_f32_16x16x32_bf16 v[136:139], a[32:35], v[152:155], v[136:139]\n\t"
    "buffer_load_dwordx4 a[120:123], %[voff], s[36:39], s56 offen offset:2048 sc1\n\t"
    "v_mfma_f32_16x16x32_bf16 v[140:143], a[32:35], v[156:159], v[140:143]\n\t"
    "s_waitcnt lgkmcnt(0)\n\t"
    "v_mfma_f32_16x16x32_bf16 v[192:195], v[200:203], v[160:163], v[192:195]\n\t"
    "v_mfma_f32_16x16x32_bf16 v[196:199], v[160:163], v[160:163], v[196:199]\n\t"
    "s_waitcnt vmcnt(24)\n\t"
    "v_mfma_f32_16x16x32_bf16 v[0:3], a[36:39], v[160:163], v[0:3]\n\t"
    "ds_read_b64_tr_b16 v[176:177], %[hb0] offset:8192\n\t"
    "v_mfma_f32_16x16x32_bf16 v[4:7], a[36:39], v[164:167], v[4:7]\n\t"
    "ds_read_b64_tr_b16 v[178:179], %[hb0] offset:8704\n\t"
    "v_mfma_f32_16x16x32_bf16 v[8:11], a[36:39], v[168:171], v[8:11]\n\t"
    "buffer_load_dwordx4 a[124:127], %[voff], s[36:39], s56 offen offset:3072 sc1\n\t"
    "v_mfma_f32_16x16x32_bf16 v[12:15], a[36:39], v[172:175], v[12:15]\n\t"
    "s_add_u32 s56, s56, 4096\n\t"
    "s_waitcnt vmcnt(24)\n\t"
    "v_mfma_f32_16x16x32_bf16 v[16:19], a[40:43], v[160:163], v[16:19]\n\t"
    "ds_read_b64_tr_b16 v[180:181], %[hb1] offset:8192\n\t"
    "v_mfma_f32_16x16x32_bf16 v[20:23], a[40:43], v[164:167], v[20:23]\n\t"
    "ds_read_b64_tr_b16 v[182:183], %[hb1] offset:8704\n\t"
    "v_mfma_f32_16x16x32_bf16 v[24:27], a[40:43], v[168:171], v[24:27]\n\t"
    "buffer_load_dwordx4 a[128:131], %[voff], s[36:39], s56 offen offset:0 sc1\n\t"
    "v_mfma_f32_16x16x32_bf16 v[28:31], a[40:43], v[172:175], v[28:31]\n\t"
    "s_waitcnt vmcnt(23)\n\t"
    "v_mfma_f32_16x16x32_bf16 v[32:35], a[44:47], v[160:163], v[32:35]\n\t"
    "ds_read_b64_tr_b16 v[184:185], %[hb2] offset:8192\n\t"
    "v_mfma_f32_16x16x32_bf16 v[36:39], a[44:47], v[164:167], v[36:39]\n\t"
    "ds_read_b64_tr_b16 v[186:187], %[hb2] offset:8704\n\t"
    "v_mfma_f32_16x16x32_bf16 v[40:43], a[44:47], v[168:171], v[40:43]\n\t"
    "buffer_load_dwordx4 a[132:135], %[voff], s[36:39], s56 offen offset:1024 sc1\n\t"
    "v_mfma_f32_16x16x32_bf16 v[44:47], a[44:47], v[172:175], v[44:47]\n\t"
    "s_waitcnt vmcnt(22)\n\t"
    "v_mfma_f32_16x16x32_bf16 v[48:51], a[48:51], v[160:163], v[48:51]\n\t"
    "ds_read_b64_tr_b16 v[188:189], %[hb3] offset:8192\n\t"
    "v_mfma_f32_16x16x32_bf16 v[52:55], a[48:51], v[164:167], v[52:55]\n\t"
    "ds_read_b64_tr_b16 v[190:191], %[hb3] offset:8704\n\t"
    "v_mfma_f32_16x16x32_bf16 v[56:59], a[48:51], v[168:171], v[56:59]\n\t"
    "buffer_load_dwordx4 a[136:139], %[voff], s[36:39], s56 offen offset:2048 sc1\n\t"
    "v_mfma_f32_16x16x32_bf16 v[60:63], a[48:51], v[172:175], v[60:63]\n\t"
    "s_waitcnt vmcnt(21)\n\t"
    "v_mfma_f32_16x16x32_bf16 v[64:67], a[52:55], v[160:163], v[64:67]\n\t"
    "v_mfma_f32_16x16x32_bf16 v[68:71], a[52:55], v[164:167], v[68:71]\n\t"
    "s_waitcnt vmcnt(26)\n\t"
    "ds_write_b128 %[hwr], a[144:147] offset:0\n\t"
    "v_mfma_f32_16x16x32_bf16 v[72:75], a[52:55], v[168:171], v[72:75]\n\t"
    "buffer_load_dwordx4 a[144:147], %[voff], s[40:43], s57 offen sc1\n\t"
    "s_add_u32 s57, s57, 0x1000\n\t"
    "v_mfma_f32_16x16x32_bf16 v[76:79], a[52:55], v[172:175], v[76:79]\n\t"
    "buffer_load_dwordx4 a[140:143], %[voff], s[36:39], s56 offen offset:3072 sc1\n\t"
    "s_waitcnt vmcnt(22)\n\t"
    "v_mfma_f32_16x16x32_bf16 v[80:83], a[56:59], v[160:163], v[80:83]\n\t"
    "s_add_u32 s56, s56, 4096\n\t"
    "v_mfma_f32_16x16x32_bf16 v[84:87], a[56:59], v[164:167], v[84:87]\n\t"
    "s_waitcnt vmcnt(26)\n\t"
    "ds_write_b128 %[hwr], a[148:151] offset:4096\n\t"
    "v_mfma_f32_16x16x32_bf16 v[88:91], a[56:59], v[168:171], v[88:91]\n\t"
    "buffer_load_dwordx4 a[148:151], %[voff], s[40:43], s57 offen sc1\n\t"
    "s_add_u32 s57, s57, 0x1000\n\t"
    "v_mfma_f32_16x16x32_bf16 v[92:95], a[56:59], v[172:175], v[92:95]\n\t"
    "buffer_load_dwordx4 a[0:3], %[voff], s[36:39], s56 offen offset:0 sc1\n\t"
    "s_waitcnt vmcnt(23)\n\t"
    "v_mfma_f32_16x16x32_bf16 v[96:99], a[60:63], v[160:163], v[96:99]\n\t"
    "v_mfma_f32_16x16x32_bf16 v[100:103], a[60:63], v[164:167], v[100:103]\n\t"
    "s_waitcnt vmcnt(26)\n\t"
    "ds_write_b128 %[hwr], a[152:155] offset:8192\n\t"
    "v_mfma_f32_16x16x32_bf16 v[104:107], a[60:63], v[168:171], v[104:107]\n\t"
    "buffer_load_dwordx4 a[152:155], %[voff], s[40:43], s57 offen sc1\n\t"
    "s_add_u32 s57, s57, 0x1000\n\t"
    "v_mfma_f32_16x16x32_bf16 v[108:111], a[60:63], v[172:175], v[108:111]\n\t"
    "s_waitcnt vmcnt(23)\n\t"
    "v_mfma_f32_16x16x32_bf16 v[112:115], a[64:67], v[160:163], v[112:115]\n\t"
    "v_mfma_f32_16x16x32_bf16 v[116:119], a[64:67], v[164:167], v[116:119]\n\t"
    "v_mfma_f32_16x16x32_bf16 v[120:123], a[64:67], v[168:171], v[120:123]\n\t"
    "buffer_load_dwordx4 a[4:7], %[voff], s[36:39], s56 offen offset:1024 sc1\n\t"
    "v_mfma_f32_16x16x32_bf16 v[124:127], a[64:67], v[172:175], v[124:127]\n\t"
    "s_waitcnt vmcnt(23)\n\t"
    "v_mfma_f32_16x16x32_bf16 v[128:131], a[68:71], v[160:163], v[128:131]\n\t"
    "v_mfma_f32_16x16x32_bf16 v[132:135], a[68:71], v[164:167], v[132:135]\n\t"
    "v_add_u32 %[hwr], 0x3000, %[hwr]\n\t"
    "v_mfma_f32_16x16x32_bf16 v[136:139], a[68:71], v[168:171], v[136:139]\n\t"
    "buffer_load_dwordx4 a[8:11], %[voff], s[36:39], s56 offen offset:2048 sc1\n\t"
    "v_mfma_f32_16x16x32_bf16 v[140:143], a[68:71], v[172:175], v[140:143]\n\t"
    "s_waitcnt lgkmcnt(0)\n\t"
    "s_barrier\n\t"
    "v_mfma_f32_16x16x32_bf16 v[192:195], v[200:203], v[176:179], v[192:195]\n\t"
    "v_mfma_f32_16x16x32_bf16 v[196:199], v[176:179], v[176:179], v[196:199]\n\t"
    "s_waitcnt vmcnt(23)\n\t"
    "v_mfma_f32_16x16x32_bf16 v[0:3], a[72:75], v[176:179], v[0:3]\n\t"
    "ds_read_b64_tr_b16 v[144:145], %[hb0] offset:12288\n\t"
    "v_mfma_f32_16x16x32_bf16 v[4:7], a[72:75], v[180:183], v[4:7]\n\t"
    "ds_read_b64_tr_b16 v[146:147], %[hb0] offset:12800\n\t"
    "v_mfma_f32_16x16x32_bf16 v[8:11], a[72:75], v[184:187], v[8:11]\n\t"
    "buffer_load_dwordx4 a[12:15], %[voff], s[36:39], s56 offen offset:3072 sc1\n\t"
    "v_mfma_f32_16x16x32_bf16 v[12:15], a[72:75], v[188:191], v[12:15]\n\t"
    "s_add_u32 s56, s56, 4096\n\t"
    "s_waitcnt vmcnt(23)\n\t"
    "v_mfma_f32_16x16x32_bf16 v[16:19], a[76:79], v[176:179], v[16:19]\n\t"
    "ds_read_b64_tr_b16 v[148:149], %[hb1] offset:12288\n\t"
    "v_mfma_f32_16x16x32_bf16 v[20:23], a[76:79], v[180:183], v[20:23]\n\t"
    "ds_read_b64_tr_b16 v[150:151], %[hb1] offset:12800\n\t"
    "v_mfma_f32_16x16x32_bf16 v[24:27], a[76:79], v[184:187], v[24:27]\n\t"
    "buffer_load_dwordx4 a[16:19], %[voff], s[36:39], s56 offen offset:0 sc1\n\t"
    "v_mfma_f32_16x16x32_bf16 v[28:31], a[76:79], v[188:191], v[28:31]\n\t"
    "s_waitcnt vmcnt(23)\n\t"
    "v_mfma_f32_16x16x32_bf16 v[32:35], a[80:83], v[176:179], v[32:35]\n\t"
    "ds_read_b64_tr_b16 v[152:153], %[hb2] offset:12288\n\t"
    "v_mfma_f32_16x16x32_bf16 v[36:39], a[80:83], v[180:183], v[36:39]\n\t"
    "ds_read_b64_tr_b16 v[154:155], %[hb2] offset:12800\n\t"
    "v_mfma_f32_16x16x32_bf16 v[40:43], a[80:83], v[184:187], v[40:43]\n\t"
    "buffer_load_dwordx4 a[20:23], %[voff], s[36:39], s56 offen offset:1024 sc1\n\t"
    "v_mfma_f32_16x16x32_bf16 v[44:47], a[80:83], v[188:191], v[44:47]\n\t"
    "s_waitcnt vmcnt(23)\n\t"
    "v_mfma_f32_16x16x32_bf16 v[48:51], a[84:87], v[176:179], v[48:51]\n\t"
    "ds_read_b64_tr_b16 v[156:157], %[hb3] offset:12288\n\t"
    "v_mfma_f32_16x16x32_bf16 v[52:55], a[84:87], v[180:183], v[52:55]\n\t"
    "ds_read_b64_tr_b16 v[158:159], %[hb3] offset:12800\n\t"
    "v_mfma_f32_16x16x32_bf16 v[56:59], a[84:87], v[184:187], v[56:59]\n\t"
    "buffer_load_dwordx4 a[24:27], %[voff], s[36:39], s56 offen offset:2048 sc1\n\t"
    "v_mfma_f32_16x16x32_bf16 v[60:63], a[84:87], v[188:191], v[60:63]\n\t"
    "s_waitcnt vmcnt(23)\n\t"
    "v_mfma_f32_16x16x32_bf16 v[64:67], a[88:91], v[176:179], v[64:67]\n\t"
    "v_mfma_f32_16x16x32_bf16 v[68:71], a[88:91], v[180:183], v[68:71]\n\t"
    "v_add_u32 %[hb0], 0x3000, %[hb0]\n\t"
    "v_mfma_f32_16x16x32_bf16 v[72:75], a[88:91], v[184:187], v[72:75]\n\t"
    "buffer_load_dwordx4 a[28:31], %[voff], s[36:39], s56 offen offset:3072 sc1\n\t"
    "v_mfma_f32_16x16x32_bf16 v[76:79], a[88:91], v[188:191], v[76:79]\n\t"
    "s_add_u32 s56, s56, 4096\n\t"
    "s_waitcnt vmcnt(23)\n\t"
    "v_mfma_f32_16x16x32_bf16 v[80:83], a[92:95], v[176:179], v[80:83]\n\t"
    "v_mfma_f32_16x16x32_bf16 v[84:87], a[92:95], v[180:183], v[84:87]\n\t"
    "v_add_u32 %[hb1], 0x3000, %[hb1]\n\t"
    "v_mfma_f32_16x16x32_bf16 v[88:91], a[92:95], v[184:187], v[88:91]\n\t"
    "v_mfma_f32_16x16x32_bf16 v[92:95], a[92:95], v[188:191], v[92:95]\n\t"
    "s_waitcnt vmcnt(22)\n\t"
    "v_mfma_f32_16x16x32_bf16 v[96:99], a[96:99], v[176:179], v[96:99]\n\t"
    "v_mfma_f32_16x16x32_bf16 v[100:103], a[96:99], v[180:183], v[100:103]\n\t"
    "v_add_u32 %[hb2], 0x3000, %[hb2]\n\t"
    "v_mfma_f32_16x16x32_bf16 v[104:107], a[96:99], v[184:187], v[104:107]\n\t"
    "buffer_load_dwordx4 a[32:35], %[voff], s[36:39], s56 offen offset:0 sc1\n\t"
    "v_mfma_f32_16x16x32_bf16 v[108:111], a[96:99], v[188:191], v[108:111]\n\t"
    "s_waitcnt vmcnt(22)\n\t"
    "v_mfma_f32_16x16x32_bf16 v[112:115], a[100:103], v[176:179], v[112:115]\n\t"
    "v_mfma_f32_16x16x32_bf16 v[116:119], a[100:103], v[180:183], v[116:119]\n\t"
    "v_add_u32 %[hb3], 0x3000, %[hb3]\n\t"
    "v_mfma_f32_16x16x32_bf16 v[120:123], a[100:103], v[184:187], v[120:123]\n\t"
    "buffer_load_dwordx4 a[36:39], %[voff], s[36:39], s56 offen offset:1024 sc1\n\t"
    "v_mfma_f32_16x16x32_bf16 v[124:127], a[100:103], v[188:191], v[124:127]\n\t"
    "s_waitcnt vmcnt(22)\n\t"
    "v_mfma_f32_16x16x32_bf16 v[128:131], a[104:107], v[176:179], v[128:131]\n\t"
    "v_mfma_f32_16x16x32_bf16 v[132:135], a[104:107], v[180:183], v[132:135]\n\t"
    "v_mfma_f32_16x16x32_bf16 v[136:139], a[104:107], v[184:187], v[136:139]\n\t"
    "buffer_load_dwordx4 a[40:43], %[voff], s[36:39], s56 offen offset:2048 sc1\n\t"
    "v_mfma_f32_16x16x32_bf16 v[140:143], a[104:107], v[188:191], v[140:143]\n\t"
    "s_waitcnt lgkmcnt(0)\n\t"
    "v_mfma_f32_16x16x32_bf16 v[192:195], v[200:203], v[144:147], v[192:195]\n\t"
    "v_mfma_f32_16x16x32_bf16 v[196:199], v[144:147], v[144:147], v[196:199]\n\t"
    "s_waitcnt vmcnt(22)\n\t"
    "v_mfma_f32_16x16x32_bf16 v[0:3], a[108:111], v[144:147], v[0:3]\n\t"
    "ds_read_b64_tr_b16 v[160:161], %[hb0] offset:4096\n\t"
    "v_mfma_f32_16x16x32_bf16 v[4:7], a[108:111], v[148:151], v[4:7]\n\t"
    "ds_read_b64_tr_b16 v[162:163], %[hb0] offset:4608\n\t"
    "v_mfma_f32_16x16x32_bf16 v[8:11], a[108:111], v[152:155], v[8:11]\n\t"
    "buffer_load_dwordx4 a[44:47], %[voff], s[36:39], s56 offen offset:3072 sc1\n\t"
    "v_mfma_f32_16x16x32_bf16 v[12:15], a[108:111], v[156:159], v[12:15]\n\t"
    "s_add_u32 s56, s56, 4096\n\t"
    "s_waitcnt vmcnt(22)\n\t"
    "v_mfma_f32_16x16x32_bf16 v[16:19], a[112:115], v[144:147], v[16:19]\n\t"
    "ds_read_b64_tr_b16 v[164:165], %[hb1] offset:4096\n\t"
    "v_mfma_f32_16x16x32_bf16 v[20:23], a[112:115], v[148:151], v[20:23]\n\t"
    "ds_read_b64_tr_b16 v[166:167], %[hb1] offset:4608\n\t"
    "v_mfma_f32_16x16x32_bf16 v[24:27], a[112:115], v[152:155], v[24:27]\n\t"
    "buffer_load_dwordx4 a[48:51], %[voff], s[36:39], s56 offen offset:0 sc1\n\t"
    "v_mfma_f32_16x16x32_bf16 v[28:31], a[112:115], v[156:159], v[28:31]\n\t"
    "s_waitcnt vmcnt(22)\n\t"
    "v_mfma_f32_16x16x32_bf16 v[32:35], a[116:119], v[144:147], v[32:35]\n\t"
    "ds_read_b64_tr_b16 v[168:169], %[hb2] offset:4096\n\t"
    "v_mfma_f32_16x16x32_bf16 v[36:39], a[116:119], v[148:151], v[36:39]\n\t"
    "ds_read_b64_tr_b16 v[170:171], %[hb2] offset:4608\n\t"
    "v_mfma_f32_16x16x32_bf16 v[40:43], a[116:119], v[152:155], v[40:43]\n\t"
    "buffer_load_dwordx4 a[52:55], %[voff], s[36:39], s56 offen offset:1024 sc1\n\t"
    "v_mfma_f32_16x16x32_bf16 v[44:47], a[116:119], v[156:159], v[44:47]\n\t"
    "s_waitcnt vmcnt(22)\n\t"
    "v_mfma_f32_16x16x32_bf16 v[48:51], a[120:123], v[144:147], v[48:51]\n\t"
    "ds_read_b64_tr_b16 v[172:173], %[hb3] offset:4096\n\t"
    "v_mfma_f32_16x16x32_bf16 v[52:55], a[120:123], v[148:151], v[52:55]\n\t"
    "ds_read_b64_tr_b16 v[174:175], %[hb3] offset:4608\n\t"
    "v_mfma_f32_16x16x32_bf16 v[56:59], a[120:123], v[152:155], v[56:59]\n\t"
    "v_mfma_f32_16x16x32_bf16 v[60:63], a[120:123], v[156:159], v[60:63]\n\t"
    "s_waitcnt vmcnt(21)\n\t"
    "v_mfma_f32_16x16x32_bf16 v[64:67], a[124:127], v[144:147], v[64:67]\n\t"
    "v_mfma_f32_16x16x32_bf16 v[68:71], a[124:127], v[148:151], v[68:71]\n\t"
    "v_mfma_f32_16x16x32_bf16 v[72:75], a[124:127], v[152:155], v[72:75]\n\t"
    "buffer_load_dwordx4 a[56:59], %[voff], s[36:39], s56 offen offset:2048 sc1\n\t"
    "v_mfma_f32_16x16x32_bf16 v[76:79], a[124:127], v[156:159], v[76:79]\n\t"
    "s_waitcnt vmcnt(21)\n\t"
    "v_mfma_f32_16x16x32_bf16 v[80:83], a[128:131], v[144:147], v[80:83]\n\t"
    "v_mfma_f32_16x16x32_bf16 v[84:87], a[128:131], v[148:151], v[84:87]\n\t"
    "v_mfma_f32_16x16x32_bf16 v[88:91], a[128:131], v[152:155], v[88:91]\n\t"
    "buffer_load_dwordx4 a[60:63], %[voff], s[36:39], s56 offen offset:3072 sc1\n\t"
    "v_mfma_f32_16x16x32_bf16 v[92:95], a[128:131], v[156:159], v[92:95]\n\t"
    "s_add_u32 s56, s56, 4096\n\t"
    "s_waitcnt vmcnt(21)\n\t"
    "v_mfma_f32_16x16x32_bf16 v[96:99], a[132:135], v[144:147], v[96:99]\n\t"
    "v_mfma_f32_16x16x32_bf16 v[100:103], a[132:135], v[148:151], v[100:103]\n\t"
    "v_mfma_f32_16x16x32_bf16 v[104:107], a[132:135], v[152:155], v[104:107]\n\t"
    "buffer_load_dwordx4 a[64:67], %[voff], s[36:39], s56 offen offset:0 sc1\n\t"
    "v_mfma_f32_16x16x32_bf16 v[108:111], a[132:135], v[156:159], v[108:111]\n\t"
    "s_waitcnt vmcnt(21)\n\t"
    "v_mfma_f32_16x16x32_bf16 v[112:115], a[136:139], v[144:147], v[112:115]\n\t"
    "v_mfma_f32_16x16x32_bf16 v[116:119], a[136:139], v[148:151], v[116:119]\n\t"
    "v_mfma_f32_16x16x32_bf16 v[120:123], a[136:139], v[152:155], v[120:123]\n\t"
    "buffer_load_dwordx4 a[68:71], %[voff], s[36:39], s56 offen offset:1024 sc1\n\t"
    "v_mfma_f32_16x16x32_bf16 v[124:127], a[136:139], v[156:159], v[124:127]\n\t"
    "s_waitcnt vmcnt(20)\n\t"
    "v_mfma_f32_16x16x32_bf16 v[128:131], a[140:143], v[144:147], v[128:131]\n\t"
    "v_mfma_f32_16x16x32_bf16 v[132:135], a[140:143], v[148:151], v[132:135]\n\t"
    "v_mfma_f32_16x16x32_bf16 v[136:139], a[140:143], v[152:155], v[136:139]\n\t"
    "buffer_load_dwordx4 a[72:75], %[voff], s[36:39], s56 offen offset:2048 sc1\n\t"
    "v_mfma_f32_16x16x32_bf16 v[140:143], a[140:143], v[156:159], v[140:143]\n\t"
    "s_waitcnt lgkmcnt(0)\n\t"
    "v_mfma_f32_16x16x32_bf16 v[192:195], v[200:203], v[160:163], v[192:195]\n\t"
    "v_mfma_f32_16x16x32_bf16 v[196:199], v[160:163], v[160:163], v[196:199]\n\t"
    "s_waitcnt vmcnt(19)\n\t"
    "v_mfma_f32_16x16x32_bf16 v[0:3], a[0:3], v[160:163], v[0:3]\n\t"
    "ds_read_b64_tr_b16 v[176:177], %[hb0] offset:8192\n\t"
    "v_mfma_f32_16x16x32_bf16 v[4:7], a[0:3], v[164:167], v[4:7]\n\t"
    "ds_read_b64_tr_b16 v[178:179], %[hb0] offset:8704\n\t"
    "v_mfma_f32_16x16x32_bf16 v[8:11], a[0:3], v[168:171], v[8:11]\n\t"
    "buffer_load_dwordx4 a[76:79], %[voff], s[36:39], s56 offen offset:3072 sc1\n\t"
    "v_mfma_f32_16x16x32_bf16 v[12:15], a[0:3], v[172:175], v[12:15]\n\t"
    "s_add_u32 s56, s56, 4096\n\t"
    "s_waitcnt vmcnt(18)\n\t"
    "v_mfma_f32_16x16x32_bf16 v[16:19], a[4:7], v[160:163], v[16:19]\n\t"
    "ds_read_b64_tr_b16 v[180:181], %[hb1] offset:8192\n\t"
    "v_mfma_f32_16x16x32_bf16 v[20:23], a[4:7], v[164:167], v[20:23]\n\t"
    "ds_read_b64_tr_b16 v[182:183], %[hb1] offset:8704\n\t"
    "v_mfma_f32_16x16x32_bf16 v[24:27], a[4:7], v[168:171], v[24:27]\n\t"
    "buffer_load_dwordx4 a[80:83], %[voff], s[36:39], s56 offen offset:0 sc1\n\t"
    "v_mfma_f32_16x16x32_bf16 v[28:31], a[4:7], v[172:175], v[28:31]\n\t"
    "s_waitcnt vmcnt(18)\n\t"
    "v_mfma_f32_16x16x32_bf16 v[32:35], a[8:11], v[160:163], v[32:35]\n\t"
    "ds_read_b64_tr_b16 v[184:185], %[hb2] offset:8192\n\t"
    "v_mfma_f32_16x16x32_bf16 v[36:39], a[8:11], v[164:167], v[36:39]\n\t"
    "ds_read_b64_tr_b16 v[186:187], %[hb2] offset:8704\n\t"
    "v_mfma_f32_16x16x32_bf16 v[40:43], a[8:11], v[168:171], v[40:43]\n\t"
    "v_mfma_f32_16x16x32_bf16 v[44:47], a[8:11], v[172:175], v[44:47]\n\t"
    "s_waitcnt vmcnt(17)\n\t"
    "v_mfma_f32_16x16x32_bf16 v[48:51], a[12:15], v[160:163], v[48:51]\n\t"
    "ds_read_b64_tr_b16 v[188:189], %[hb3] offset:8192\n\t"
    "v_mfma_f32_16x16x32_bf16 v[52:55], a[12:15], v[164:167], v[52:55]\n\t"
    "ds_read_b64_tr_b16 v[190:191], %[hb3] offset:8704\n\t"
    "v_mfma_f32_16x16x32_bf16 v[56:59], a[12:15], v[168:171], v[56:59]\n\t"
    "buffer_load_dwordx4 a[84:87], %[voff], s[36:39], s56 offen offset:1024 sc1\n\t"
    "v_mfma_f32_16x16x32_bf16 v[60:63], a[12:15], v[172:175], v[60:63]\n\t"
    "s_waitcnt vmcnt(17)\n\t"
    "v_mfma_f32_16x16x32_bf16 v[64:67], a[16:19], v[160:163], v[64:67]\n\t"
    "v_mfma_f32_16x16x32_bf16 v[68:71], a[16:19], v[164:167], v[68:71]\n\t"
    "s_waitcnt vmcnt(25)\n\t"
    "ds_write_b128 %[hwr], a[144:147] offset:0\n\t"
    "v_mfma_f32_16x16x32_bf16 v[72:75], a[16:19], v[168:171], v[72:75]\n\t"
    "buffer_load_dwordx4 a[144:147], %[voff], s[40:43], s57 offen sc1\n\t"
    "s_add_u32 s57, s57, 0x1000\n\t"
    "v_mfma_f32_16x16x32_bf16 v[76:79], a[16:19], v[172:175], v[76:79]\n\t"
    "buffer_load_dwordx4 a[88:91], %[voff], s[36:39], s56 offen offset:2048 sc1\n\t"
    "s_waitcnt vmcnt(18)\n\t"
    "v_mfma_f32_16x16x32_bf16 v[80:83], a[20:23], v[160:163], v[80:83]\n\t"
    "v_mfma_f32_16x16x32_bf16 v[84:87], a[20:23], v[164:167], v[84:87]\n\t"
    "s_waitcnt vmcnt(25)\n\t"
    "ds_write_b128 %[hwr], a[148:151] offset:4096\n\t"
    "v_mfma_f32_16x16x32_bf16 v[88:91], a[20:23], v[168:171], v[88:91]\n\t"
    "buffer_load_dwordx4 a[148:151], %[voff], s[40:43], s57 offen sc1\n\t"
    "s_add_u32 s57, s57, 0x1000\n\t"
    "v_mfma_f32_16x16x32_bf16 v[92:95], a[20:23], v[172:175], v[92:95]\n\t"
    "buffer_load_dwordx4 a[92:95], %[voff], s[36:39], s56 offen offset:3072 sc1\n\t"
    "s_waitcnt vmcnt(19)\n\t"
    "v_mfma_f32_16x16x32_bf16 v[96:99], a[24:27], v[160:163], v[96:99]\n\t"
    "s_add_u32 s56, s56, 4096\n\t"
    "v_mfma_f32_16x16x32_bf16 v[100:103], a[24:27], v[164:167], v[100:103]\n\t"
    "s_waitcnt vmcnt(25)\n\t"
    "ds_write_b128 %[hwr], a[152:155] offset:8192\n\t"
    "v_mfma_f32_16x16x32_bf16 v[104:107], a[24:27], v[168:171], v[104:107]\n\t"
    "buffer_load_dwordx4 a[152:155], %[voff], s[40:43], s57 offen sc1\n\t"
    "s_add_u32 s57, s57, 0x1000\n\t"
    "v_mfma_f32_16x16x32_bf16 v[108:111], a[24:27], v[172:175], v[108:111]\n\t"
    "buffer_load_dwordx4 a[96:99], %[voff], s[36:39], s56 offen offset:0 sc1\n\t"
    "s_waitcnt vmcnt(20)\n\t"
    "v_mfma_f32_16x16x32_bf16 v[112:115], a[28:31], v[160:163], v[112:115]\n\t"
    "v_mfma_f32_16x16x32_bf16 v[116:119], a[28:31], v[164:167], v[116:119]\n\t"
    "v_mfma_f32_16x16x32_bf16 v[120:123], a[28:31], v[168:171], v[120:123]\n\t"
    "buffer_load_dwordx4 a[100:103], %[voff], s[36:39], s56 offen offset:1024 sc1\n\t"
    "v_mfma_f32_16x16x32_bf16 v[124:127], a[28:31], v[172:175], v[124:127]\n\t"
    "s_waitcnt vmcnt(20)\n\t"
    "v_mfma_f32_16x16x32_bf16 v[128:131], a[32:35], v[160:163], v[128:131]\n\t"
    "v_mfma_f32_16x16x32_bf16 v[132:135], a[32:35], v[164:167], v[132:135]\n\t"
    "v_add_u32 %[hwr], 0x3000, %[hwr]\n\t"
    "v_mfma_f32_16x16x32_bf16 v[136:139], a[32:35], v[168:171], v[136:139]\n\t"
    "buffer_load_dwordx4 a[104:107], %[voff], s[36:39], s56 offen offset:2048 sc1\n\t"
    "v_mfma_f32_16x16x32_bf16 v[140:143], a[32:35], v[172:175], v[140:143]\n\t"
    "s_waitcnt lgkmcnt(0)\n\t"
    "s_barrier\n\t"
    "v_mfma_f32_16x16x32_bf16 v[192:195], v[200:203], v[176:179], v[192:195]\n\t"
    "v_mfma_f32_16x16x32_bf16 v[196:199], v[176:179], v[176:179], v[196:199]\n\t"
    "s_waitcnt vmcnt(20)\n\t"
    "v_mfma_f32_16x16x32_bf16 v[0:3], a[36:39], v[176:179], v[0:3]\n\t"
    "ds_read_b64_tr_b16 v[144:145], %[hb0] offset:12288\n\t"
    "v_mfma_f32_16x16x32_bf16 v[4:7], a[36:39], v[180:183], v[4:7]\n\t"
    "ds_read_b64_tr_b16 v[146:147], %[hb0] offset:12800\n\t"
    "v_mfma_f32_16x16x32_bf16 v[8:11], a[36:39], v[184:187], v[8:11]\n\t"
    "buffer_load_dwordx4 a[108:111], %[voff], s[36:39], s56 offen offset:3072 sc1\n\t"
    "v_mfma_f32_16x16x32_bf16 v[12:15], a[36:39], v[188:191], v[12:15]\n\t"
    "s_add_u32 s56, s56, 4096\n\t"
    "s_waitcnt vmcnt(20)\n\t"
    "v_mfma_f32_16x16x32_bf16 v[16:19], a[40:43], v[176:179], v[16:19]\n\t"
    "ds_read_b64_tr_b16 v[148:149], %[hb1] offset:12288\n\t"
    "v_mfma_f32_16x16x32_bf16 v[20:23], a[40:43], v[180:183], v[20:23]\n\t"
    "ds_read_b64_tr_b16 v[150:151], %[hb1] offset:12800\n\t"
    "v_mfma_f32_16x16x32_bf16 v[24:27], a[40:43], v[184:187], v[24:27]\n\t"
    "v_mfma_f32_16x16x32_bf16 v[28:31], a[40:43], v[188:191], v[28:31]\n\t"
    "s_waitcnt vmcnt(19)\n\t"
    "v_mfma_f32_16x16x32_bf16 v[32:35], a[44:47], v[176:179], v[32:35]\n\t"
    "ds_read_b64_tr_b16 v[152:153], %[hb2] offset:12288\n\t"
    "v_mfma_f32_16x16x32_bf16 v[36:39], a[44:47], v[180:183], v[36:39]\n\t"
    "ds_read_b64_tr_b16 v[154:155], %[hb2] offset:12800\n\t"
    "v_mfma_f32_16x16x32_bf16 v[40:43], a[44:47], v[184:187], v[40:43]\n\t"
    "buffer_load_dwordx4 a[112:115], %[voff], s[36:39], s56 offen offset:0 sc1\n\t"
    "v_mfma_f32_16x16x32_bf16 v[44:47], a[44:47], v[188:191], v[44:47]\n\t"
    "s_waitcnt vmcnt(19)\n\t"
    "v_mfma_f32_16x16x32_bf16 v[48:51], a[48:51], v[176:179], v[48:51]\n\t"
    "ds_read_b64_tr_b16 v[156:157], %[hb3] offset:12288\n\t"
    "v_mfma_f32_16x16x32_bf16 v[52:55], a[48:51], v[180:183], v[52:55]\n\t"
    "ds_read_b64_tr_b16 v[158:159], %[hb3] offset:12800\n\t"
    "v_mfma_f32_16x16x32_bf16 v[56:59], a[48:51], v[184:187], v[56:59]\n\t"
    "buffer_load_dwordx4 a[116:119], %[voff], s[36:39], s56 offen offset:1024 sc1\n\t"
    "v_mfma_f32_16x16x32_bf16 v[60:63], a[48:51], v[188:191], v[60:63]\n\t"
    "s_waitcnt vmcnt(19)\n\t"
    "v_mfma_f32_16x16x32_bf16 v[64:67], a[52:55], v[176:179], v[64:67]\n\t"
    "v_mfma_f32_16x16x32_bf16 v[68:71], a[52:55], v[180:183], v[68:71]\n\t"
    "v_add_u32 %[hb0], 0x3000, %[hb0]\n\t"
    "v_mfma_f32_16x16x32_bf16 v[72:75], a[52:55], v[184:187], v[72:75]\n\t"
    "buffer_load_dwordx4 a[120:123], %[voff], s[36:39], s56 offen offset:2048 sc1\n\t"
    "v_mfma_f32_16x16x32_bf16 v[76:79], a[52:55], v[188:191], v[76:79]\n\t"
    "s_waitcnt vmcnt(19)\n\t"
    "v_mfma_f32_16x16x32_bf16 v[80:83], a[56:59], v[176:179], v[80:83]\n\t"
    "v_mfma_f32_16x16x32_bf16 v[84:87], a[56:59], v[180:183], v[84:87]\n\t"
    "v_add_u32 %[hb1], 0x3000, %[hb1]\n\t"
    "v_mfma_f32_16x16x32_bf16 v[88:91], a[56:59], v[184:187], v[88:91]\n\t"
    "buffer_load_dwordx4 a[124:127], %[voff], s[36:39], s56 offen offset:3072 sc1\n\t"
    "v_mfma_f32_16x16x32_bf16 v[92:95], a[56:59], v[188:191], v[92:95]\n\t"
    "s_add_u32 s56, s56, 4096\n\t"
    "s_waitcnt vmcnt(19)\n\t"
    "v_mfma_f32_16x16x32_bf16 v[96:99], a[60:63], v[176:179], v[96:99]\n\t"
    "v_mfma_f32_16x16x32_bf16 v[100:103], a[60:63], v[180:183], v[100:103]\n\t"
    "v_add_u32 %[hb2], 0x3000, %[hb2]\n\t"
    "v_mfma_f32_16x16x32_bf16 v[104:107], a[60:63], v[184:187], v[104:107]\n\t"
    "buffer_load_dwordx4 a[128:131], %[voff], s[36:39], s56 offen offset:0 sc1\n\t"
    "v_mfma_f32_16x16x32_bf16 v[108:111], a[60:63], v[188:191], v[108:111]\n\t"
    "s_waitcnt vmcnt(19)\n\t"
    "v_mfma_f32_16x16x32_bf16 v[112:115], a[64:67], v[176:179], v[112:115]\n\t"
    "v_mfma_f32_16x16x32_bf16 v[116:119], a[64:67], v[180:183], v[116:119]\n\t"
    "v_add_u32 %[hb3], 0x3000, %[hb3]\n\t"
    "v_mfma_f32_16x16x32_bf16 v[120:123], a[64:67], v[184:187], v[120:123]\n\t"
    "buffer_load_dwordx4 a[132:135], %[voff], s[36:39], s56 offen offset:1024 sc1\n\t"
    "v_mfma_f32_16x16x32_bf16 v[124:127], a[64:67], v[188:191], v[124:127]\n\t"
    "s_waitcnt vmcnt(19)\n\t"
    "v_mfma_f32_16x16x32_bf16 v[128:131], a[68:71], v[176:179], v[128:131]\n\t"
    "v_mfma_f32_16x16x32_bf16 v[132:135], a[68:71], v[180:183], v[132:135]\n\t"
    "v_mfma_f32_16x16x32_bf16 v[136:139], a[68:71], v[184:187], v[136:139]\n\t"
    "v_mfma_f32_16x16x32_bf16 v[140:143], a[68:71], v[188:191], v[140:143]\n\t"
    "s_waitcnt lgkmcnt(0)\n\t"
    "v_mfma_f32_16x16x32_bf16 v[192:195], v[200:203], v[144:147], v[192:195]\n\t"
    "v_mfma_f32_16x16x32_bf16 v[196:199], v[144:147], v[144:147], v[196:199]\n\t"
    "s_waitcnt vmcnt(18)\n\t"
    "v_mfma_f32_16x16x32_bf16 v[0:3], a[72:75], v[144:147], v[0:3]\n\t"
    "ds_read_b64_tr_b16 v[160:161], %[hb0] offset:4096\n\t"
    "v_mfma_f32_16x16x32_bf16 v[4:7], a[72:75], v[148:151], v[4:7]\n\t"
    "ds_read_b64_tr_b16 v[162:163], %[hb0] offset:4608\n\t"
    "v_mfma_f32_16x16x32_bf16 v[8:11], a[72:75], v[152:155], v[8:11]\n\t"
    "buffer_load_dwordx4 a[136:139], %[voff], s[36:39], s56 offen offset:2048 sc1\n\t"
    "v_mfma_f32_16x16x32_bf16 v[12:15], a[72:75], v[156:159], v[12:15]\n\t"
    "s_waitcnt vmcnt(18)\n\t"
    "v_mfma_f32_16x16x32_bf16 v[16:19], a[76:79], v[144:147], v[16:19]\n\t"
    "ds_read_b64_tr_b16 v[164:165], %[hb1] offset:4096\n\t"
    "v_mfma_f32_16x16x32_bf16 v[20:23], a[76:79], v[148:151], v[20:23]\n\t"
    "ds_read_b64_tr_b16 v[166:167], %[hb1] offset:4608\n\t"
    "v_mfma_f32_16x16x32_bf16 v[24:27], a[76:79], v[152:155], v[24:27]\n\t"
    "buffer_load_dwordx4 a[140:143], %[voff], s[36:39], s56 offen offset:3072 sc1\n\t"
    "v_mfma_f32_16x16x32_bf16 v[28:31], a[76:79], v[156:159], v[28:31]\n\t"
    "s_add_u32 s56, s56, 4096\n\t"
    "s_waitcnt vmcnt(18)\n\t"
    "v_mfma_f32_16x16x32_bf16 v[32:35], a[80:83], v[144:147], v[32:35]\n\t"
    "ds_read_b64_tr_b16 v[168:169], %[hb2] offset:4096\n\t"
    "v_mfma_f32_16x16x32_bf16 v[36:39], a[80:83], v[148:151], v[36:39]\n\t"
    "ds_read_b64_tr_b16 v[170:171], %[hb2] offset:4608\n\t"
    "v_mfma_f32_16x16x32_bf16 v[40:43], a[80:83], v[152:155], v[40:43]\n\t"
    "buffer_load_dwordx4 a[0:3], %[voff], s[36:39], s56 offen offset:0 sc1\n\t"
    "v_mfma_f32_16x16x32_bf16 v[44:47], a[80:83], v[156:159], v[44:47]\n\t"
    "s_waitcnt vmcnt(18)\n\t"
    "v_mfma_f32_16x16x32_bf16 v[48:51], a[84:87], v[144:147], v[48:51]\n\t"
    "ds_read_b64_tr_b16 v[172:173], %[hb3] offset:4096\n\t"
    "v_mfma_f32_16x16x32_bf16 v[52:55], a[84:87], v[148:151], v[52:55]\n\t"
    "ds_read_b64_tr_b16 v[174:175], %[hb3] offset:4608\n\t"
    "v_mfma_f32_16x16x32_bf16 v[56:59], a[84:87], v[152:155], v[56:59]\n\t"
    "buffer_load_dwordx4 a[4:7], %[voff], s[36:39], s56 offen offset:1024 sc1\n\t"
    "v_mfma_f32_16x16x32_bf16 v[60:63], a[84:87], v[156:159], v[60:63]\n\t"
    "s_waitcnt vmcnt(17)\n\t"
    "v_mfma_f32_16x16x32_bf16 v[64:67], a[88:91], v[144:147], v[64:67]\n\t"
    "v_mfma_f32_16x16x32_bf16 v[68:71], a[88:91], v[148:151], v[68:71]\n\t"
    "v_mfma_f32_16x16x32_bf16 v[72:75], a[88:91], v[152:155], v[72:75]\n\t"
    "buffer_load_dwordx4 a[8:11], %[voff], s[36:39], s56 offen offset:2048 sc1\n\t"
    "v_mfma_f32_16x16x32_bf16 v[76:79], a[88:91], v[156:159], v[76:79]\n\t"
    "s_waitcnt vmcnt(16)\n\t"
    "v_mfma_f32_16x16x32_bf16 v[80:83], a[92:95], v[144:147], v[80:83]\n\t"
    "v_mfma_f32_16x16x32_bf16 v[84:87], a[92:95], v[148:151], v[84:87]\n\t"
    "v_mfma_f32_16x16x32_bf16 v[88:91], a[92:95], v[152:155], v[88:91]\n\t"
    "buffer_load_dwordx4 a[12:15], %[voff], s[36:39], s56 offen offset:3072 sc1\n\t"
    "v_mfma_f32_16x16x32_bf16 v[92:95], a[92:95], v[156:159], v[92:95]\n\t"
    "s_add_u32 s56, s56, 4096\n\t"
    "s_waitcnt vmcnt(15)\n\t"
    "v_mfma_f32_16x16x32_bf16 v[96:99], a[96:99], v[144:147], v[96:99]\n\t"
    "v_mfma_f32_16x16x32_bf16 v[100:103], a[96:99], v[148:151], v[100:103]\n\t"
    "v_mfma_f32_16x16x32_bf16 v[104:107], a[96:99], v[152:155], v[104:107]\n\t"
    "buffer_load_dwordx4 a[16:19], %[voff], s[36:39], s56 offen offset:0 sc1\n\t"
    "v_mfma_f32_16x16x32_bf16 v[108:111], a[96:99], v[156:159], v[108:111]\n\t"
    "s_waitcnt vmcnt(15)\n\t"
    "v_mfma_f32_16x16x32_bf16 v[112:115], a[100:103], v[144:147], v[112:115]\n\t"
    "v_mfma_f32_16x16x32_bf16 v[116:119], a[100:103], v[148:151], v[116:119]\n\t"
    "v_mfma_f32_16x16x32_bf16 v[120:123], a[100:103], v[152:155], v[120:123]\n\t"
    "v_mfma_f32_16x16x32_bf16 v[124:127], a[100:103], v[156:159], v[124:127]\n\t"
    "s_waitcnt vmcnt(14)\n\t"
    "v_mfma_f32_16x16x32_bf16 v[128:131], a[104:107], v[144:147], v[128:131]\n\t"
    "v_mfma_f32_16x16x32_bf16 v[132:135], a[104:107], v[148:151], v[132:135]\n\t"
    "v_mfma_f32_16x16x32_bf16 v[136:139], a[104:107], v[152:155], v[136:139]\n\t"
    "buffer_load_dwordx4 a[20:23], %[voff], s[36:39], s56 offen offset:1024 sc1\n\t"
    "v_mfma_f32_16x16x32_bf16 v[140:143], a[104:107], v[156:159], v[140:143]\n\t"
    "s_waitcnt lgkmcnt(0)\n\t"
    "v_mfma_f32_16x16x32_bf16 v[192:195], v[200:203], v[160:163], v[192:195]\n\t"
    "v_mfma_f32_16x16x32_bf16 v[196:199], v[160:163], v[160:163], v[196:199]\n\t"
    "s_waitcnt vmcnt(14)\n\t"
    "v_mfma_f32_16x16x32_bf16 v[0:3], a[108:111], v[160:163], v[0:3]\n\t"
    "ds_read_b64_tr_b16 v[176:177], %[hb0] offset:8192\n\t"
    "v_mfma_f32_16x16x32_bf16 v[4:7], a[108:111], v[164:167], v[4:7]\n\t"
    "ds_read_b64_tr_b16 v[178:179], %[hb0] offset:8704\n\t"
    "v_mfma_f32_16x16x32_bf16 v[8:11], a[108:111], v[168:171], v[8:11]\n\t"
    "buffer_load_dwordx4 a[24:27], %[voff], s[36:39], s56 offen offset:2048 sc1\n\t"
    "v_mfma_f32_16x16x32_bf16 v[12:15], a[108:111], v[172:175], v[12:15]\n\t"
    "s_waitcnt vmcnt(14)\n\t"
    "v_mfma_f32_16x16x32_bf16 v[16:19], a[112:115], v[160:163], v[16:19]\n\t"
    "ds_read_b64_tr_b16 v[180:181], %[hb1] offset:8192\n\t"
    "v_mfma_f32_16x16x32_bf16 v[20:23], a[112:115], v[164:167], v[20:23]\n\t"
    "ds_read_b64_tr_b16 v[182:183], %[hb1] offset:8704\n\t"
    "v_mfma_f32_16x16x32_bf16 v[24:27], a[112:115], v[168:171], v[24:27]\n\t"
    "buffer_load_dwordx4 a[28:31], %[voff], s[36:39], s56 offen offset:3072 sc1\n\t"
    "v_mfma_f32_16x16x32_bf16 v[28:31], a[112:115], v[172:175], v[28:31]\n\t"
    "s_add_u32 s56, s56, 4096\n\t"
    "s_waitcnt vmcnt(14)\n\t"
    "v_mfma_f32_16x16x32_bf16 v[32:35], a[116:119], v[160:163], v[32:35]\n\t"
    "ds_read_b64_tr_b16 v[184:185], %[hb2] offset:8192\n\t"
    "v_mfma_f32_16x16x32_bf16 v[36:39], a[116:119], v[164:167], v[36:39]\n\t"
    "ds_read_b64_tr_b16 v[186:187], %[hb2] offset:8704\n\t"
    "v_mfma_f32_16x16x32_bf16 v[40:43], a[116:119], v[168:171], v[40:43]\n\t"
    "buffer_load_dwordx4 a[32:35], %[voff], s[36:39], s56 offen offset:0 sc1\n\t"
    "v_mfma_f32_16x16x32_bf16 v[44:47], a[116:119], v[172:175], v[44:47]\n\t"
    "s_waitcnt vmcnt(14)\n\t"
    "v_mfma_f32_16x16x32_bf16 v[48:51], a[120:123], v[160:163], v[48:51]\n\t"
    "ds_read_b64_tr_b16 v[188:189], %[hb3] offset:8192\n\t"
    "v_mfma_f32_16x16x32_bf16 v[52:55], a[120:123], v[164:167], v[52:55]\n\t"
    "ds_read_b64_tr_b16 v[190:191], %[hb3] offset:8704\n\t"
    "v_mfma_f32_16x16x32_bf16 v[56:59], a[120:123], v[168:171], v[56:59]\n\t"
    "buffer_load_dwordx4 a[36:39], %[voff], s[36:39], s56 offen offset:1024 sc1\n\t"
    "v_mfma_f32_16x16x32_bf16 v[60:63], a[120:123], v[172:175], v[60:63]\n\t"
    "s_waitcnt vmcnt(14)\n\t"
    "v_mfma_f32_16x16x32_bf16 v[64:67], a[124:127], v[160:163], v[64:67]\n\t"
    "v_mfma_f32_16x16x32_bf16 v[68:71], a[124:127], v[164:167], v[68:71]\n\t"
    "s_waitcnt vmcnt(26)\n\t"
    "ds_write_b128 %[hwr], a[144:147] offset:0\n\t"
    "v_mfma_f32_16x16x32_bf16 v[72:75], a[124:127], v[168:171], v[72:75]\n\t"
    "buffer_load_dwordx4 a[40:43], %[voff], s[36:39], s56 offen offset:2048 sc1\n\t"
    "v_mfma_f32_16x16x32_bf16 v[76:79], a[124:127], v[172:175], v[76:79]\n\t"
    "s_waitcnt vmcnt(14)\n\t"
    "v_mfma_f32_16x16x32_bf16 v[80:83], a[128:131], v[160:163], v[80:83]\n\t"
    "v_mfma_f32_16x16x32_bf16 v[84:87], a[128:131], v[164:167], v[84:87]\n\t"
    "s_waitcnt vmcnt(25)\n\t"
    "ds_write_b128 %[hwr], a[148:151] offset:4096\n\t"
    "v_mfma_f32_16x16x32_bf16 v[88:91], a[128:131], v[168:171], v[88:91]\n\t"
    "buffer_load_dwordx4 a[44:47], %[voff], s[36:39], s56 offen offset:3072 sc1\n\t"
    "v_mfma_f32_16x16x32_bf16 v[92:95], a[128:131], v[172:175], v[92:95]\n\t"
    "s_add_u32 s56, s56, 4096\n\t"
    "s_waitcnt vmcnt(14)\n\t"
    "v_mfma_f32_16x16x32_bf16 v[96:99], a[132:135], v[160:163], v[96:99]\n\t"
    "v_mfma_f32_16x16x32_bf16 v[100:103], a[132:135], v[164:167], v[100:103]\n\t"
    "s_waitcnt vmcnt(24)\n\t"
    "ds_write_b128 %[hwr], a[152:155] offset:8192\n\t"
    "v_mfma_f32_16x16x32_bf16 v[104:107], a[132:135], v[168:171], v[104:107]\n\t"
    "v_mfma_f32_16x16x32_bf16 v[108:111], a[132:135], v[172:175], v[108:111]\n\t"
    "s_waitcnt vmcnt(13)\n\t"
    "v_mfma_f32_16x16x32_bf16 v[112:115], a[136:139], v[160:163], v[112:115]\n\t"
    "v_mfma_f32_16x16x32_bf16 v[116:119], a[136:139], v[164:167], v[116:119]\n\t"
    "v_mfma_f32_16x16x32_bf16 v[120:123], a[136:139], v[168:171], v[120:123]\n\t"
    "buffer_load_dwordx4 a[48:51], %[voff], s[36:39], s56 offen offset:0 sc1\n\t"
    "v_mfma_f32_16x16x32_bf16 v[124:127], a[136:139], v[172:175], v[124:127]\n\t"
    "s_waitcnt vmcnt(13)\n\t"
    "v_mfma_f32_16x16x32_bf16 v[128:131], a[140:143], v[160:163], v[128:131]\n\t"
    "v_mfma_f32_16x16x32_bf16 v[132:135], a[140:143], v[164:167], v[132:135]\n\t"
    "v_mfma_f32_16x16x32_bf16 v[136:139], a[140:143], v[168:171], v[136:139]\n\t"
    "buffer_load_dwordx4 a[52:55], %[voff], s[36:39], s56 offen offset:1024 sc1\n\t"
    "v_mfma_f32_16x16x32_bf16 v[140:143], a[140:143], v[172:175], v[140:143]\n\t"
    "s_waitcnt lgkmcnt(0)\n\t"
    "s_barrier\n\t"
    "v_mfma_f32_16x16x32_bf16 v[192:195], v[200:203], v[176:179], v[192:195]\n\t"
    "v_mfma_f32_16x16x32_bf16 v[196:199], v[176:179], v[176:179], v[196:199]\n\t"
    "s_waitcnt vmcnt(13)\n\t"
    "v_mfma_f32_16x16x32_bf16 v[0:3], a[0:3], v[176:179], v[0:3]\n\t"
    "ds_read_b64_tr_b16 v[144:145], %[hb0] offset:12288\n\t"
    "v_mfma_f32_16x16x32_bf16 v[4:7], a[0:3], v[180:183], v[4:7]\n\t"
    "ds_read_b64_tr_b16 v[146:147], %[hb0] offset:12800\n\t"
    "v_mfma_f32_16x16x32_bf16 v[8:11], a[0:3], v[184:187], v[8:11]\n\t"
    "buffer_load_dwordx4 a[56:59], %[voff], s[36:39], s56 offen offset:2048 sc1\n\t"
    "v_mfma_f32_16x16x32_bf16 v[12:15], a[0:3], v[188:191], v[12:15]\n\t"
    "s_waitcnt vmcnt(13)\n\t"
    "v_mfma_f32_16x16x32_bf16 v[16:19], a[4:7], v[176:179], v[16:19]\n\t"
    "ds_read_b64_tr_b16 v[148:149], %[hb1] offset:12288\n\t"
    "v_mfma_f32_16x16x32_bf16 v[20:23], a[4:7], v[180:183], v[20:23]\n\t"
    "ds_read_b64_tr_b16 v[150:151], %[hb1] offset:12800\n\t"
    "v_mfma_f32_16x16x32_bf16 v[24:27], a[4:7], v[184:187], v[24:27]\n\t"
    "buffer_load_dwordx4 a[60:63], %[voff], s[36:39], s56 offen offset:3072 sc1\n\t"
    "v_mfma_f32_16x16x32_bf16 v[28:31], a[4:7], v[188:191], v[28:31]\n\t"
    "s_add_u32 s56, s56, 4096\n\t"
    "s_waitcnt vmcnt(13)\n\t"
    "v_mfma_f32_16x16x32_bf16 v[32:35], a[8:11], v[176:179], v[32:35]\n\t"
    "ds_read_b64_tr_b16 v[152:153], %[hb2] offset:12288\n\t"
    "v_mfma_f32_16x16x32_bf16 v[36:39], a[8:11], v[180:183], v[36:39]\n\t"
    "ds_read_b64_tr_b16 v[154:155], %[hb2] offset:12800\n\t"
    "v_mfma_f32_16x16x32_bf16 v[40:43], a[8:11], v[184:187], v[40:43]\n\t"
    "buffer_load_dwordx4 a[64:67], %[voff], s[36:39], s56 offen offset:0 sc1\n\t"
    "v_mfma_f32_16x16x32_bf16 v[44:47], a[8:11], v[188:191], v[44:47]\n\t"
    "s_waitcnt vmcnt(13)\n\t"
    "v_mfma_f32_16x16x32_bf16 v[48:51], a[12:15], v[176:179], v[48:51]\n\t"
    "ds_read_b64_tr_b16 v[156:157], %[hb3] offset:12288\n\t"
    "v_mfma_f32_16x16x32_bf16 v[52:55], a[12:15], v[180:183], v[52:55]\n\t"
    "ds_read_b64_tr_b16 v[158:159], %[hb3] offset:12800\n\t"
    "v_mfma_f32_16x16x32_bf16 v[56:59], a[12:15], v[184:187], v[56:59]\n\t"
    "buffer_load_dwordx4 a[68:71], %[voff], s[36:39], s56 offen offset:1024 sc1\n\t"
    "v_mfma_f32_16x16x32_bf16 v[60:63], a[12:15], v[188:191], v[60:63]\n\t"
    "s_waitcnt vmcnt(13)\n\t"
    "v_mfma_f32_16x16x32_bf16 v[64:67], a[16:19], v[176:179], v[64:67]\n\t"
    "v_mfma_f32_16x16x32_bf16 v[68:71], a[16:19], v[180:183], v[68:71]\n\t"
    "v_add_u32 %[hb0], 0x3000, %[hb0]\n\t"
    "v_mfma_f32_16x16x32_bf16 v[72:75], a[16:19], v[184:187], v[72:75]\n\t"
    "buffer_load_dwordx4 a[72:75], %[voff], s[36:39], s56 offen offset:2048 sc1\n\t"
    "v_mfma_f32_16x16x32_bf16 v[76:79], a[16:19], v[188:191], v[76:79]\n\t"
    "s_waitcnt vmcnt(13)\n\t"
    "v_mfma_f32_16x16x32_bf16 v[80:83], a[20:23], v[176:179], v[80:83]\n\t"
    "v_mfma_f32_16x16x32_bf16 v[84:87], a[20:23], v[180:183], v[84:87]\n\t"
    "v_add_u32 %[hb1], 0x3000, %[hb1]\n\t"
    "v_mfma_f32_16x16x32_bf16 v[88:91], a[20:23], v[184:187], v[88:91]\n\t"
    "v_mfma_f32_16x16x32_bf16 v[92:95], a[20:23], v[188:191], v[92:95]\n\t"
    "s_waitcnt vmcnt(12)\n\t"
    "v_mfma_f32_16x16x32_bf16 v[96:99], a[24:27], v[176:179], v[96:99]\n\t"
    "v_mfma_f32_16x16x32_bf16 v[100:103], a[24:27], v[180:183], v[100:103]\n\t"
    "v_add_u32 %[hb2], 0x3000, %[hb2]\n\t"
    "v_mfma_f32_16x16x32_bf16 v[104:107], a[24:27], v[184:187], v[104:107]\n\t"
    "buffer_load_dwordx4 a[76:79], %[voff], s[36:39], s56 offen offset:3072 sc1\n\t"
    "v_mfma_f32_16x16x32_bf16 v[108:111], a[24:27], v[188:191], v[108:111]\n\t"
    "s_add_u32 s56, s56, 4096\n\t"
    "s_waitcnt vmcnt(12)\n\t"
    "v_mfma_f32_16x16x32_bf16 v[112:115], a[28:31], v[176:179], v[112:115]\n\t"
    "v_mfma_f32_16x16x32_bf16 v[116:119], a[28:31], v[180:183], v[116:119]\n\t"
    "v_add_u32 %[hb3], 0x3000, %[hb3]\n\t"
    "v_mfma_f32_16x16x32_bf16 v[120:123], a[28:31], v[184:187], v[120:123]\n\t"
    "buffer_load_dwordx4 a[80:83], %[voff], s[36:39], s56 offen offset:0 sc1\n\t"
    "v_mfma_f32_16x16x32_bf16 v[124:127], a[28:31], v[188:191], v[124:127]\n\t"
    "s_waitcnt vmcnt(12)\n\t"
    "v_mfma_f32_16x16x32_bf16 v[128:131], a[32:35], v[176:179], v[128:131]\n\t"
    "v_mfma_f32_16x16x32_bf16 v[132:135], a[32:35], v[180:183], v[132:135]\n\t"
    "v_mfma_f32_16x16x32_bf16 v[136:139], a[32:35], v[184:187], v[136:139]\n\t"
    "buffer_load_dwordx4 a[84:87], %[voff], s[36:39], s56 offen offset:1024 sc1\n\t"
    "v_mfma_f32_16x16x32_bf16 v[140:143], a[32:35], v[188:191], v[140:143]\n\t"
    "s_waitcnt lgkmcnt(0)\n\t"
    "v_mfma_f32_16x16x32_bf16 v[192:195], v[200:203], v[144:147], v[192:195]\n\t"
    "v_mfma_f32_16x16x32_bf16 v[196:199], v[144:147], v[144:147], v[196:199]\n\t"
    "s_waitcnt vmcnt(12)\n\t"
    "v_mfma_f32_16x16x32_bf16 v[0:3], a[36:39], v[144:147], v[0:3]\n\t"
    "ds_read_b64_tr_b16 v[160:161], %[hb0] offset:4096\n\t"
    "v_mfma_f32_16x16x32_bf16 v[4:7], a[36:39], v[148:151], v[4:7]\n\t"
    "ds_read_b64_tr_b16 v[162:163], %[hb0] offset:4608\n\t"
    "v_mfma_f32_16x16x32_bf16 v[8:11], a[36:39], v[152:155], v[8:11]\n\t"
    "buffer_load_dwordx4 a[88:91], %[voff], s[36:39], s56 offen offset:2048 sc1\n\t"
    "v_mfma_f32_16x16x32_bf16 v[12:15], a[36:39], v[156:159], v[12:15]\n\t"
    "s_waitcnt vmcnt(12)\n\t"
    "v_mfma_f32_16x16x32_bf16 v[16:19], a[40:43], v[144:147], v[16:19]\n\t"
    "ds_read_b64_tr_b16 v[164:165], %[hb1] offset:4096\n\t"
    "v_mfma_f32_16x16x32_bf16 v[20:23], a[40:43], v[148:151], v[20:23]\n\t"
    "ds_read_b64_tr_b16 v[166:167], %[hb1] offset:4608\n\t"
    "v_mfma_f32_16x16x32_bf16 v[24:27], a[40:43], v[152:155], v[24:27]\n\t"
    "buffer_load_dwordx4 a[92:95], %[voff], s[36:39], s56 offen offset:3072 sc1\n\t"
    "v_mfma_f32_16x16x32_bf16 v[28:31], a[40:43], v[156:159], v[28:31]\n\t"
    "s_add_u32 s56, s56, 4096\n\t"
    "s_waitcnt vmcnt(12)\n\t"
    "v_mfma_f32_16x16x32_bf16 v[32:35], a[44:47], v[144:147], v[32:35]\n\t"
    "ds_read_b64_tr_b16 v[168:169], %[hb2] offset:4096\n\t"
    "v_mfma_f32_16x16x32_bf16 v[36:39], a[44:47], v[148:151], v[36:39]\n\t"
    "ds_read_b64_tr_b16 v[170:171], %[hb2] offset:4608\n\t"
    "v_mfma_f32_16x16x32_bf16 v[40:43], a[44:47], v[152:155], v[40:43]\n\t"
    "buffer_load_dwordx4 a[96:99], %[voff], s[36:39], s56 offen offset:0 sc1\n\t"
    "v_mfma_f32_16x16x32_bf16 v[44:47], a[44:47], v[156:159], v[44:47]\n\t"
    "s_waitcnt vmcnt(12)\n\t"
    "v_mfma_f32_16x16x32_bf16 v[48:51], a[48:51], v[144:147], v[48:51]\n\t"
    "ds_read_b64_tr_b16 v[172:173], %[hb3] offset:4096\n\t"
    "v_mfma_f32_16x16x32_bf16 v[52:55], a[48:51], v[148:151], v[52:55]\n\t"
    "ds_read_b64_tr_b16 v[174:175], %[hb3] offset:4608\n\t"
    "v_mfma_f32_16x16x32_bf16 v[56:59], a[48:51], v[152:155], v[56:59]\n\t"
    "v_mfma_f32_16x16x32_bf16 v[60:63], a[48:51], v[156:159], v[60:63]\n\t"
    "s_waitcnt vmcnt(11)\n\t"
    "v_mfma_f32_16x16x32_bf16 v[64:67], a[52:55], v[144:147], v[64:67]\n\t"
    "v_mfma_f32_16x16x32_bf16 v[68:71], a[52:55], v[148:151], v[68:71]\n\t"
    "v_mfma_f32_16x16x32_bf16 v[72:75], a[52:55], v[152:155], v[72:75]\n\t"
    "buffer_load_dwordx4 a[100:103], %[voff], s[36:39], s56 offen offset:1024 sc1\n\t"
    "v_mfma_f32_16x16x32_bf16 v[76:79], a[52:55], v[156:159], v[76:79]\n\t"
    "s_waitcnt vmcnt(11)\n\t"
    "v_mfma_f32_16x16x32_bf16 v[80:83], a[56:59], v[144:147], v[80:83]\n\t"
    "v_mfma_f32_16x16x32_bf16 v[84:87], a[56:59], v[148:151], v[84:87]\n\t"
    "v_mfma_f32_16x16x32_bf16 v[88:91], a[56:59], v[152:155], v[88:91]\n\t"
    "buffer_load_dwordx4 a[104:107], %[voff], s[36:39], s56 offen offset:2048 sc1\n\t"
    "v_mfma_f32_16x16x32_bf16 v[92:95], a[56:59], v[156:159], v[92:95]\n\t"
    "s_waitcnt vmcnt(11)\n\t"
    "v_mfma_f32_16x16x32_bf16 v[96:99], a[60:63], v[144:147], v[96:99]\n\t"
    "v_mfma_f32_16x16x32_bf16 v[100:103], a[60:63], v[148:151], v[100:103]\n\t"
    "v_mfma_f32_16x16x32_bf16 v[104:107], a[60:63], v[152:155], v[104:107]\n\t"
    "buffer_load_dwordx4 a[108:111], %[voff], s[36:39], s56 offen offset:3072 sc1\n\t"
    "v_mfma_f32_16x16x32_bf16 v[108:111], a[60:63], v[156:159], v[108:111]\n\t"
    "s_add_u32 s56, s56, 4096\n\t"
    "s_waitcnt vmcnt(11)\n\t"
    "v_mfma_f32_16x16x32_bf16 v[112:115], a[64:67], v[144:147], v[112:115]\n\t"
    "v_mfma_f32_16x16x32_bf16 v[116:119], a[64:67], v[148:151], v[116:119]\n\t"
    "v_mfma_f32_16x16x32_bf16 v[120:123], a[64:67], v[152:155], v[120:123]\n\t"
    "buffer_load_dwordx4 a[112:115], %[voff], s[36:39], s56 offen offset:0 sc1\n\t"
    "v_mfma_f32_16x16x32_bf16 v[124:127], a[64:67], v[156:159], v[124:127]\n\t"
    "s_waitcnt vmcnt(11)\n\t"
    "v_mfma_f32_16x16x32_bf16 v[128:131], a[68:71], v[144:147], v[128:131]\n\t"
    "v_mfma_f32_16x16x32_bf16 v[132:135], a[68:71], v[148:151], v[132:135]\n\t"
    "v_mfma_f32_16x16x32_bf16 v[136:139], a[68:71], v[152:155], v[136:139]\n\t"
    "buffer_load_dwordx4 a[116:119], %[voff], s[36:39], s56 offen offset:1024 sc1\n\t"
    "v_mfma_f32_16x16x32_bf16 v[140:143], a[68:71], v[156:159], v[140:143]\n\t"
    "s_waitcnt lgkmcnt(0)\n\t"
    "v_mfma_f32_16x16x32_bf16 v[192:195], v[200:203], v[160:163], v[192:195]\n\t"
    "v_mfma_f32_16x16x32_bf16 v[196:199], v[160:163], v[160:163], v[196:199]\n\t"
    "s_waitcnt vmcnt(11)\n\t"
    "v_mfma_f32_16x16x32_bf16 v[0:3], a[72:75], v[160:163], v[0:3]\n\t"
    "ds_read_b64_tr_b16 v[176:177], %[hb0] offset:8192\n\t"
    "v_mfma_f32_16x16x32_bf16 v[4:7], a[72:75], v[164:167], v[4:7]\n\t"
    "ds_read_b64_tr_b16 v[178:179], %[hb0] offset:8704\n\t"
    "v_mfma_f32_16x16x32_bf16 v[8:11], a[72:75], v[168:171], v[8:11]\n\t"
    "buffer_load_dwordx4 a[120:123], %[voff], s[36:39], s56 offen offset:2048 sc1\n\t"
    "v_mfma_f32_16x16x32_bf16 v[12:15], a[72:75], v[172:175], v[12:15]\n\t"
    "s_waitcnt vmcnt(11)\n\t"
    "v_mfma_f32_16x16x32_bf16 v[16:19], a[76:79], v[160:163], v[16:19]\n\t"
    "ds_read_b64_tr_b16 v[180:181], %[hb1] offset:8192\n\t"
    "v_mfma_f32_16x16x32_bf16 v[20:23], a[76:79], v[164:167], v[20:23]\n\t"
    "ds_read_b64_tr_b16 v[182:183], %[hb1] offset:8704\n\t"
    "v_mfma_f32_16x16x32_bf16 v[24:27], a[76:79], v[168:171], v[24:27]\n\t"
    "buffer_load_dwordx4 a[124:127], %[voff], s[36:39], s56 offen offset:3072 sc1\n\t"
    "v_mfma_f32_16x16x32_bf16 v[28:31], a[76:79], v[172:175], v[28:31]\n\t"
    "s_add_u32 s56, s56, 4096\n\t"
    "s_waitcnt vmcnt(11)\n\t"
    "v_mfma_f32_16x16x32_bf16 v[32:35], a[80:83], v[160:163], v[32:35]\n\t"
    "ds_read_b64_tr_b16 v[184:185], %[hb2] offset:8192\n\t"
    "v_mfma_f32_16x16x32_bf16 v[36:39], a[80:83], v[164:167], v[36:39]\n\t"
    "ds_read_b64_tr_b16 v[186:187], %[hb2] offset:8704\n\t"
    "v_mfma_f32_16x16x32_bf16 v[40:43], a[80:83], v[168:171], v[40:43]\n\t"
    "v_mfma_f32_16x16x32_bf16 v[44:47], a[80:83], v[172:175], v[44:47]\n\t"
    "s_waitcnt vmcnt(10)\n\t"
    "v_mfma_f32_16x16x32_bf16 v[48:51], a[84:87], v[160:163], v[48:51]\n\t"
    "ds_read_b64_tr_b16 v[188:189], %[hb3] offset:8192\n\t"
    "v_mfma_f32_16x16x32_bf16 v[52:55], a[84:87], v[164:167], v[52:55]\n\t"
    "ds_read_b64_tr_b16 v[190:191], %[hb3] offset:8704\n\t"
    "v_mfma_f32_16x16x32_bf16 v[56:59], a[84:87], v[168:171], v[56:59]\n\t"
    "buffer_load_dwordx4 a[128:131], %[voff], s[36:39], s56 offen offset:0 sc1\n\t"
    "v_mfma_f32_16x16x32_bf16 v[60:63], a[84:87], v[172:175], v[60:63]\n\t"
    "s_waitcnt vmcnt(10)\n\t"
    "v_mfma_f32_16x16x32_bf16 v[64:67], a[88:91], v[160:163], v[64:67]\n\t"
    "v_mfma_f32_16x16x32_bf16 v[68:71], a[88:91], v[164:167], v[68:71]\n\t"
    "v_mfma_f32_16x16x32_bf16 v[72:75], a[88:91], v[168:171], v[72:75]\n\t"
    "buffer_load_dwordx4 a[132:135], %[voff], s[36:39], s56 offen offset:1024 sc1\n\t"
    "v_mfma_f32_16x16x32_bf16 v[76:79], a[88:91], v[172:175], v[76:79]\n\t"
    "s_waitcnt vmcnt(10)\n\t"
    "v_mfma_f32_16x16x32_bf16 v[80:83], a[92:95], v[160:163], v[80:83]\n\t"
    "v_mfma_f32_16x16x32_bf16 v[84:87], a[92:95], v[164:167], v[84:87]\n\t"
    "v_mfma_f32_16x16x32_bf16 v[88:91], a[92:95], v[168:171], v[88:91]\n\t"
    "buffer_load_dwordx4 a[136:139], %[voff], s[36:39], s56 offen offset:2048 sc1\n\t"
    "v_mfma_f32_16x16x32_bf16 v[92:95], a[92:95], v[172:175], v[92:95]\n\t"
    "s_waitcnt vmcnt(10)\n\t"
    "v_mfma_f32_16x16x32_bf16 v[96:99], a[96:99], v[160:163], v[96:99]\n\t"
    "v_mfma_f32_16x16x32_bf16 v[100:103], a[96:99], v[164:167], v[100:103]\n\t"
    "v_mfma_f32_16x16x32_bf16 v[104:107], a[96:99], v[168:171], v[104:107]\n\t"
    "buffer_load_dwordx4 a[140:143], %[voff], s[36:39], s56 offen offset:3072 sc1\n\t"
    "v_mfma_f32_16x16x32_bf16 v[108:111], a[96:99], v[172:175], v[108:111]\n\t"
    "s_add_u32 s56, s56, 4096\n\t"
    "s_waitcnt vmcnt(10)\n\t"
    "v_mfma_f32_16x16x32_bf16 v[112:115], a[100:103], v[160:163], v[112:115]\n\t"
    "v_mfma_f32_16x16x32_bf16 v[116:119], a[100:103], v[164:167], v[116:119]\n\t"
    "v_mfma_f32_16x16x32_bf16 v[120:123], a[100:103], v[168:171], v[120:123]\n\t"
    "buffer_load_dwordx4 a[0:3], %[voff], s[36:39], s56 offen offset:0 sc1\n\t"
    "v_mfma_f32_16x16x32_bf16 v[124:127], a[100:103], v[172:175], v[124:127]\n\t"
    "s_waitcnt vmcnt(10)\n\t"
    "v_mfma_f32_16x16x32_bf16 v[128:131], a[104:107], v[160:163], v[128:131]\n\t"
    "v_mfma_f32_16x16x32_bf16 v[132:135], a[104:107], v[164:167], v[132:135]\n\t"
    "v_mfma_f32_16x16x32_bf16 v[136:139], a[104:107], v[168:171], v[136:139]\n\t"
    "buffer_load_dwordx4 a[4:7], %[voff], s[36:39], s56 offen offset:1024 sc1\n\t"
    "v_mfma_f32_16x16x32_bf16 v[140:143], a[104:107], v[172:175], v[140:143]\n\t"
    "s_waitcnt lgkmcnt(0)\n\t"
    "v_mfma_f32_16x16x32_bf16 v[192:195], v[200:203], v[176:179], v[192:195]\n\t"
    "v_mfma_f32_16x16x32_bf16 v[196:199], v[176:179], v[176:179], v[196:199]\n\t"
    "s_waitcnt vmcnt(10)\n\t"
    "v_mfma_f32_16x16x32_bf16 v[0:3], a[108:111], v[176:179], v[0:3]\n\t"
    "v_mfma_f32_16x16x32_bf16 v[4:7], a[108:111], v[180:183], v[4:7]\n\t"
    "v_mfma_f32_16x16x32_bf16 v[8:11], a[108:111], v[184:187], v[8:11]\n\t"
    "buffer_load_dwordx4 a[8:11], %[voff], s[36:39], s56 offen offset:2048 sc1\n\t"
    "v_mfma_f32_16x16x32_bf16 v[12:15], a[108:111], v[188:191], v[12:15]\n\t"
    "s_waitcnt vmcnt(10)\n\t"
    "v_mfma_f32_16x16x32_bf16 v[16:19], a[112:115], v[176:179], v[16:19]\n\t"
    "v_mfma_f32_16x16x32_bf16 v[20:23], a[112:115], v[180:183], v[20:23]\n\t"
    "v_mfma_f32_16x16x32_bf16 v[24:27], a[112:115], v[184:187], v[24:27]\n\t"
    "v_mfma_f32_16x16x32_bf16 v[28:31], a[112:115], v[188:191], v[28:31]\n\t"
    "s_waitcnt vmcnt(9)\n\t"
    "v_mfma_f32_16x16x32_bf16 v[32:35], a[116:119], v[176:179], v[32:35]\n\t"
    "v_mfma_f32_16x16x32_bf16 v[36:39], a[116:119], v[180:183], v[36:39]\n\t"
    "v_mfma_f32_16x16x32_bf16 v[40:43], a[116:119], v[184:187], v[40:43]\n\t"
    "buffer_load_dwordx4 a[12:15], %[voff], s[36:39], s56 offen offset:3072 sc1\n\t"
    "v_mfma_f32_16x16x32_bf16 v[44:47], a[116:119], v[188:191], v[44:47]\n\t"
    "s_add_u32 s56, s56, 4096\n\t"
    "s_waitcnt vmcnt(9)\n\t"
    "v_mfma_f32_16x16x32_bf16 v[48:51], a[120:123], v[176:179], v[48:51]\n\t"
    "v_mfma_f32_16x16x32_bf16 v[52:55], a[120:123], v[180:183], v[52:55]\n\t"
    "v_mfma_f32_16x16x32_bf16 v[56:59], a[120:123], v[184:187], v[56:59]\n\t"
    "buffer_load_dwordx4 a[16:19], %[voff], s[36:39], s56 offen offset:0 sc1\n\t"
    "v_mfma_f32_16x16x32_bf16 v[60:63], a[120:123], v[188:191], v[60:63]\n\t"
    "s_waitcnt vmcnt(9)\n\t"
    "v_mfma_f32_16x16x32_bf16 v[64:67], a[124:127], v[176:179], v[64:67]\n\t"
    "v_mfma_f32_16x16x32_bf16 v[68:71], a[124:127], v[180:183], v[68:71]\n\t"
    "v_mfma_f32_16x16x32_bf16 v[72:75], a[124:127], v[184:187], v[72:75]\n\t"
    "buffer_load_dwordx4 a[20:23], %[voff], s[36:39], s56 offen offset:1024 sc1\n\t"
    "v_mfma_f32_16x16x32_bf16 v[76:79], a[124:127], v[188:191], v[76:79]\n\t"
    "s_waitcnt vmcnt(9)\n\t"
    "v_mfma_f32_16x16x32_bf16 v[80:83], a[128:131], v[176:179], v[80:83]\n\t"
    "v_mfma_f32_16x16x32_bf16 v[84:87], a[128:131], v[180:183], v[84:87]\n\t"
    "v_mfma_f32_16x16x32_bf16 v[88:91], a[128:131], v[184:187], v[88:91]\n\t"
    "buffer_load_dwordx4 a[24:27], %[voff], s[36:39], s56 offen offset:2048 sc1\n\t"
    "v_mfma_f32_16x16x32_bf16 v[92:95], a[128:131], v[188:191], v[92:95]\n\t"
    "s_waitcnt vmcnt(9)\n\t"
    "v_mfma_f32_16x16x32_bf16 v[96:99], a[132:135], v[176:179], v[96:99]\n\t"
    "v_mfma_f32_16x16x32_bf16 v[100:103], a[132:135], v[180:183], v[100:103]\n\t"
    "v_mfma_f32_16x16x32_bf16 v[104:107], a[132:135], v[184:187], v[104:107]\n\t"
    "buffer_load_dwordx4 a[28:31], %[voff], s[36:39], s56 offen offset:3072 sc1\n\t"
    "v_mfma_f32_16x16x32_bf16 v[108:111], a[132:135], v[188:191], v[108:111]\n\t"
    "s_add_u32 s56, s56, 4096\n\t"
    "s_waitcnt vmcnt(9)\n\t"
    "v_mfma_f32_16x16x32_bf16 v[112:115], a[136:139], v[176:179], v[112:115]\n\t"
    "v_mfma_f32_16x16x32_bf16 v[116:119], a[136:139], v[180:183], v[116:119]\n\t"
    "v_mfma_f32_16x16x32_bf16 v[120:123], a[136:139], v[184:187], v[120:123]\n\t"
    "buffer_load_dwordx4 a[32:35], %[voff], s[36:39], s56 offen offset:0 sc1\n\t"
    "v_mfma_f32_16x16x32_bf16 v[124:127], a[136:139], v[188:191], v[124:127]\n\t"
    "s_waitcnt vmcnt(9)\n\t"
    "v_mfma_f32_16x16x32_bf16 v[128:131], a[140:143], v[176:179], v[128:131]\n\t"
    "v_mfma_f32_16x16x32_bf16 v[132:135], a[140:143], v[180:183], v[132:135]\n\t"
    "v_mfma_f32_16x16x32_bf16 v[136:139], a[140:143], v[184:187], v[136:139]\n\t"
    "v_mfma_f32_16x16x32_bf16 v[140:143], a[140:143], v[188:191], v[140:143]\n\t"
    "v_mul_f32 v204, 0x3ae38e39, v192\n\t"
    "v_cvt_pk_bf16_f32 v205, v204, v204\n\t"
    "v_and_b32 v205, 0xffff0000, v205\n\t"
    "ds_write2st64_b32 %[stw], v205, v205 offset0:0 offset1:1\n\t"
    "buffer_load_dwordx4 a[36:39], %[voff], s[36:39], s56 offen offset:1024 sc1\n\t"
    "buffer_load_dwordx4 a[40:43], %[voff], s[36:39], s56 offen offset:2048 sc1\n\t"
    "buffer_load_dwordx4 a[44:47], %[voff], s[36:39], s56 offen offset:3072 sc1\n\t"
    "s_add_u32 s56, s56, 4096\n\t"
    "s_waitcnt lgkmcnt(0)\n\t"
    "s_barrier\n\t"
    "ds_read_b32 v180, %[stw] offset:0\n\t"
    "ds_read_b32 v182, %[stw] offset:64\n\t"
    "ds_read_b32 v184, %[stw] offset:128\n\t"
    "ds_read_b32 v186, %[stw] offset:192\n\t"
    "ds_read_b128 v[144:147], %[csr] offset:0\n\t"
    "ds_read_b128 v[148:151], %[csr] offset:64\n\t"
    "ds_read_b128 v[152:155], %[csr] offset:128\n\t"
    "ds_read_b128 v[156:159], %[csr] offset:192\n\t"
    "ds_read_b128 v[160:163], %[csr] offset:256\n\t"
    "ds_read_b128 v[164:167], %[csr] offset:320\n\t"
    "ds_read_b128 v[168:171], %[csr] offset:384\n\t"
    "ds_read_b128 v[172:175], %[csr] offset:448\n\t"
    "ds_read_b128 v[176:179], %[csr] offset:512\n\t"
    "v_cmp_eq_u32 vcc, 1, %[p4]\n\t"
    "s_nop 1\n\t"
    "v_cndmask_b32 v206, v196, v197, vcc\n\t"
    "v_cmp_eq_u32 vcc, 2, %[p4]\n\t"
    "s_nop 1\n\t"
    "v_cndmask_b32 v206, v206, v198, vcc\n\t"
    "v_cmp_eq_u32 vcc, 3, %[p4]\n\t"
    "s_nop 1\n\t"
    "v_cndmask_b32 v206, v206, v199, vcc\n\t"
    "buffer_load_dwordx4 a[48:51], %[voff], s[36:39], s56 offen offset:0 sc1\n\t"
    "v_mul_f32 v206, 0x3ae38e39, v206\n\t"
    "v_fma_f32 v207, -v204, v204, v206\n\t"
    "v_max_f32 v207, 0, v207\n\t"
    "v_cvt_pk_bf16_f32 v207, v207, v207\n\t"
    "v_and_b32 v207, 0xffff0000, v207\n\t"
    "v_add_f32 v207, 0x3727c5ac, v207\n\t"
    "v_cvt_pk_bf16_f32 v207, v207, v207\n\t"
    "v_and_b32 v207, 0xffff0000, v207\n\t"
    "v_sqrt_f32 v207, v207\n\t"
    "s_nop 0\n\t"
    "v_cvt_pk_bf16_f32 v207, v207, v207\n\t"
    "v_and_b32 v207, 0xffff0000, v207\n\t"
    "v_rcp_f32 v207, v207\n\t"
    "s_nop 0\n\t"
    "ds_write_b32 %[rso], v207\n\t"
    "s_waitcnt lgkmcnt(0)\n\t"
    "v_pk_fma_f32 v[0:1], v[180:181], v[144:145], v[0:1] op_sel_hi:[0,1,1] neg_lo:[1,0,0] neg_hi:[1,0,0]\n\t"
    "v_pk_fma_f32 v[2:3], v[180:181], v[146:147], v[2:3] op_sel_hi:[0,1,1] neg_lo:[1,0,0] neg_hi:[1,0,0]\n\t"
    "v_pk_fma_f32 v[4:5], v[182:183], v[144:145], v[4:5] op_sel_hi:[0,1,1] neg_lo:[1,0,0] neg_hi:[1,0,0]\n\t"
    "v_pk_fma_f32 v[6:7], v[182:183], v[146:147], v[6:7] op_sel_hi:[0,1,1] neg_lo:[1,0,0] neg_hi:[1,0,0]\n\t"
    "v_cvt_pk_bf16_f32 v0, v0, v1\n\t"
    "v_cvt_pk_bf16_f32 v1, v2, v3\n\t"
    "v_cvt_pk_bf16_f32 v4, v4, v5\n\t"
    "v_cvt_pk_bf16_f32 v5, v6, v7\n\t"
    "v_pk_max_i16 v0, v0, 0\n\t"
    "v_pk_max_i16 v1, v1, 0\n\t"
    "v_pk_max_i16 v4, v4, 0\n\t"
    "v_pk_max_i16 v5, v5, 0\n\t"
    "ds_write_b64 %[Hw0], v[0:1] offset:0\n\t"
    "ds_write_b64 %[Hw1], v[4:5] offset:0\n\t"
    "buffer_load_dwordx4 a[52:55], %[voff], s[36:39], s56 offen offset:1024 sc1\n\t"
    "buffer_load_dwordx4 a[56:59], %[voff], s[36:39], s56 offen offset:2048 sc1\n\t"
    "v_pk_fma_f32 v[8:9], v[184:185], v[144:145], v[8:9] op_sel_hi:[0,1,1] neg_lo:[1,0,0] neg_hi:[1,0,0]\n\t"
    "v_pk_fma_f32 v[10:11], v[184:185], v[146:147], v[10:11] op_sel_hi:[0,1,1] neg_lo:[1,0,0] neg_hi:[1,0,0]\n\t"
    "v_pk_fma_f32 v[12:13], v[186:187], v[144:145], v[12:13] op_sel_hi:[0,1,1] neg_lo:[1,0,0] neg_hi:[1,0,0]\n\t"
    "v_pk_fma_f32 v[14:15], v[186:187], v[146:147], v[14:15] op_sel_hi:[0,1,1] neg_lo:[1,0,0] neg_hi:[1,0,0]\n\t"
    "v_cvt_pk_bf16_f32 v8, v8, v9\n\t"
    "v_cvt_pk_bf16_f32 v9, v10, v11\n\t"
    "v_cvt_pk_bf16_f32 v12, v12, v13\n\t"
    "v_cvt_pk_bf16_f32 v13, v14, v15\n\t"
    "v_pk_max_i16 v8, v8, 0\n\t"
    "v_pk_max_i16 v9, v9, 0\n\t"
    "v_pk_max_i16 v12, v12, 0\n\t"
    "v_pk_max_i16 v13, v13, 0\n\t"
    "ds_write_b64 %[Hw2], v[8:9] offset:0\n\t"
    "ds_write_b64 %[Hw3], v[12:13] offset:0\n\t"
    "buffer_load_dwordx4 a[60:63], %[voff], s[36:39], s56 offen offset:3072 sc1\n\t"
    "s_add_u32 s56, s56, 4096\n\t"
    "buffer_load_dwordx4 a[64:67], %[voff], s[36:39], s56 offen offset:0 sc1\n\t"
    "v_pk_fma_f32 v[16:17], v[180:181], v[148:149], v[16:17] op_sel_hi:[0,1,1] neg_lo:[1,0,0] neg_hi:[1,0,0]\n\t"
    "v_pk_fma_f32 v[18:19], v[180:181], v[150:151], v[18:19] op_sel_hi:[0,1,1] neg_lo:[1,0,0] neg_hi:[1,0,0]\n\t"
    "v_pk_fma_f32 v[20:21], v[182:183], v[148:149], v[20:21] op_sel_hi:[0,1,1] neg_lo:[1,0,0] neg_hi:[1,0,0]\n\t"
    "v_pk_fma_f32 v[22:23], v[182:183], v[150:151], v[22:23] op_sel_hi:[0,1,1] neg_lo:[1,0,0] neg_hi:[1,0,0]\n\t"
    "v_cvt_pk_bf16_f32 v16, v16, v17\n\t"
    "v_cvt_pk_bf16_f32 v17, v18, v19\n\t"
    "v_cvt_pk_bf16_f32 v20, v20, v21\n\t"
    "v_cvt_pk_bf16_f32 v21, v22, v23\n\t"
    "v_pk_max_i16 v16, v16, 0\n\t"
    "v_pk_max_i16 v17, v17, 0\n\t"
    "v_pk_max_i16 v20, v20, 0\n\t"
    "v_pk_max_i16 v21, v21, 0\n\t"
    "ds_write_b64 %[Hw0], v[16:17] offset:512\n\t"
    "ds_write_b64 %[Hw1], v[20:21] offset:512\n\t"
    "buffer_load_dwordx4 a[68:71], %[voff], s[36:39], s56 offen offset:1024 sc1\n\t"
    "buffer_load_dwordx4 a[72:75], %[voff], s[36:39], s56 offen offset:2048 sc1\n\t"
    "v_pk_fma_f32 v[24:25], v[184:185], v[148:149], v[24:25] op_sel_hi:[0,1,1] neg_lo:[1,0,0] neg_hi:[1,0,0]\n\t"
    "v_pk_fma_f32 v[26:27], v[184:185], v[150:151], v[26:27] op_sel_hi:[0,1,1] neg_lo:[1,0,0] neg_hi:[1,0,0]\n\t"
    "v_pk_fma_f32 v[28:29], v[186:187], v[148:149], v[28:29] op_sel_hi:[0,1,1] neg_lo:[1,0,0] neg_hi:[1,0,0]\n\t"
    "v_pk_fma_f32 v[30:31], v[186:187], v[150:151], v[30:31] op_sel_hi:[0,1,1] neg_lo:[1,0,0] neg_hi:[1,0,0]\n\t"
    "v_cvt_pk_bf16_f32 v24, v24, v25\n\t"
    "v_cvt_pk_bf16_f32 v25, v26, v27\n\t"
    "v_cvt_pk_bf16_f32 v28, v28, v29\n\t"
    "v_cvt_pk_bf16_f32 v29, v30, v31\n\t"
    "v_pk_max_i16 v24, v24, 0\n\t"
    "v_pk_max_i16 v25, v25, 0\n\t"
    "v_pk_max_i16 v28, v28, 0\n\t"
    "v_pk_max_i16 v29, v29, 0\n\t"
    "ds_write_b64 %[Hw2], v[24:25] offset:512\n\t"
    "ds_write_b64 %[Hw3], v[28:29] offset:512\n\t"
    "buffer_load_dwordx4 a[76:79], %[voff], s[36:39], s56 offen offset:3072 sc1\n\t"
    "s_add_u32 s56, s56, 4096\n\t"
    "buffer_load_dwordx4 a[80:83], %[voff], s[36:39], s56 offen offset:0 sc1\n\t"
    "v_pk_fma_f32 v[32:33], v[180:181], v[152:153], v[32:33] op_sel_hi:[0,1,1] neg_lo:[1,0,0] neg_hi:[1,0,0]\n\t"
    "v_pk_fma_f32 v[34:35], v[180:181], v[154:155], v[34:35] op_sel_hi:[0,1,1] neg_lo:[1,0,0] neg_hi:[1,0,0]\n\t"
    "v_pk_fma_f32 v[36:37], v[182:183], v[152:153], v[36:37] op_sel_hi:[0,1,1] neg_lo:[1,0,0] neg_hi:[1,0,0]\n\t"
    "v_pk_fma_f32 v[38:39], v[182:183], v[154:155], v[38:39] op_sel_hi:[0,1,1] neg_lo:[1,0,0] neg_hi:[1,0,0]\n\t"
    "v_cvt_pk_bf16_f32 v32, v32, v33\n\t"
    "v_cvt_pk_bf16_f32 v33, v34, v35\n\t"
    "v_cvt_pk_bf16_f32 v36, v36, v37\n\t"
    "v_cvt_pk_bf16_f32 v37, v38, v39\n\t"
    "v_pk_max_i16 v32, v32, 0\n\t"
    "v_pk_max_i16 v33, v33, 0\n\t"
    "v_pk_max_i16 v36, v36, 0\n\t"
    "v_pk_max_i16 v37, v37, 0\n\t"
    "ds_write_b64 %[Hw0], v[32:33] offset:1024\n\t"
    "ds_write_b64 %[Hw1], v[36:37] offset:1024\n\t"
    "buffer_load_dwordx4 a[84:87], %[voff], s[36:39], s56 offen offset:1024 sc1\n\t"
    "buffer_load_dwordx4 a[88:91], %[voff], s[36:39], s56 offen offset:2048 sc1\n\t"
    "v_pk_fma_f32 v[40:41], v[184:185], v[152:153], v[40:41] op_sel_hi:[0,1,1] neg_lo:[1,0,0] neg_hi:[1,0,0]\n\t"
    "v_pk_fma_f32 v[42:43], v[184:185], v[154:155], v[42:43] op_sel_hi:[0,1,1] neg_lo:[1,0,0] neg_hi:[1,0,0]\n\t"
    "v_pk_fma_f32 v[44:45], v[186:187], v[152:153], v[44:45] op_sel_hi:[0,1,1] neg_lo:[1,0,0] neg_hi:[1,0,0]\n\t"
    "v_pk_fma_f32 v[46:47], v[186:187], v[154:155], v[46:47] op_sel_hi:[0,1,1] neg_lo:[1,0,0] neg_hi:[1,0,0]\n\t"
    "v_cvt_pk_bf16_f32 v40, v40, v41\n\t"
    "v_cvt_pk_bf16_f32 v41, v42, v43\n\t"
    "v_cvt_pk_bf16_f32 v44, v44, v45\n\t"
    "v_cvt_pk_bf16_f32 v45, v46, v47\n\t"
    "v_pk_max_i16 v40, v40, 0\n\t"
    "v_pk_max_i16 v41, v41, 0\n\t"
    "v_pk_max_i16 v44, v44, 0\n\t"
    "v_pk_max_i16 v45, v45, 0\n\t"
    "ds_write_b64 %[Hw2], v[40:41] offset:1024\n\t"
    "ds_write_b64 %[Hw3], v[44:45] offset:1024\n\t"
    "buffer_load_dwordx4 a[92:95], %[voff], s[36:39], s56 offen offset:3072 sc1\n\t"
    "s_add_u32 s56, s56, 4096\n\t"
    "buffer_load_dwordx4 a[96:99], %[voff], s[36:39], s56 offen offset:0 sc1\n\t"
    "v_pk_fma_f32 v[48:49], v[180:181], v[156:157], v[48:49] op_sel_hi:[0,1,1] neg_lo:[1,0,0] neg_hi:[1,0,0]\n\t"
    "v_pk_fma_f32 v[50:51], v[180:181], v[158:159], v[50:51] op_sel_hi:[0,1,1] neg_lo:[1,0,0] neg_hi:[1,0,0]\n\t"
    "v_pk_fma_f32 v[52:53], v[182:183], v[156:157], v[52:53] op_sel_hi:[0,1,1] neg_lo:[1,0,0] neg_hi:[1,0,0]\n\t"
    "v_pk_fma_f32 v[54:55], v[182:183], v[158:159], v[54:55] op_sel_hi:[0,1,1] neg_lo:[1,0,0] neg_hi:[1,0,0]\n\t"
    "v_cvt_pk_bf16_f32 v48, v48, v49\n\t"
    "v_cvt_pk_bf16_f32 v49, v50, v51\n\t"
    "v_cvt_pk_bf16_f32 v52, v52, v53\n\t"
    "v_cvt_pk_bf16_f32 v53, v54, v55\n\t"
    "v_pk_max_i16 v48, v48, 0\n\t"
    "v_pk_max_i16 v49, v49, 0\n\t"
    "v_pk_max_i16 v52, v52, 0\n\t"
    "v_pk_max_i16 v53, v53, 0\n\t"
    "ds_write_b64 %[Hw0], v[48:49] offset:1536\n\t"
    "ds_write_b64 %[Hw1], v[52:53] offset:1536\n\t"
    "buffer_load_dwordx4 a[100:103], %[voff], s[36:39], s56 offen offset:1024 sc1\n\t"
    "v_pk_fma_f32 v[56:57], v[184:185], v[156:157], v[56:57] op_sel_hi:[0,1,1] neg_lo:[1,0,0] neg_hi:[1,0,0]\n\t"
    "v_pk_fma_f32 v[58:59], v[184:185], v[158:159], v[58:59] op_sel_hi:[0,1,1] neg_lo:[1,0,0] neg_hi:[1,0,0]\n\t"
    "v_pk_fma_f32 v[60:61], v[186:187], v[156:157], v[60:61] op_sel_hi:[0,1,1] neg_lo:[1,0,0] neg_hi:[1,0,0]\n\t"
    "v_pk_fma_f32 v[62:63], v[186:187], v[158:159], v[62:63] op_sel_hi:[0,1,1] neg_lo:[1,0,0] neg_hi:[1,0,0]\n\t"
    "v_cvt_pk_bf16_f32 v56, v56, v57\n\t"
    "v_cvt_pk_bf16_f32 v57, v58, v59\n\t"
    "v_cvt_pk_bf16_f32 v60, v60, v61\n\t"
    "v_cvt_pk_bf16_f32 v61, v62, v63\n\t"
    "v_pk_max_i16 v56, v56, 0\n\t"
    "v_pk_max_i16 v57, v57, 0\n\t"
    "v_pk_max_i16 v60, v60, 0\n\t"
    "v_pk_max_i16 v61, v61, 0\n\t"
    "ds_write_b64 %[Hw2], v[56:57] offset:1536\n\t"
    "ds_write_b64 %[Hw3], v[60:61] offset:1536\n\t"
    "buffer_load_dwordx4 a[104:107], %[voff], s[36:39], s56 offen offset:2048 sc1\n\t"
    "v_pk_fma_f32 v[64:65], v[180:181], v[160:161], v[64:65] op_sel_hi:[0,1,1] neg_lo:[1,0,0] neg_hi:[1,0,0]\n\t"
    "v_pk_fma_f32 v[66:67], v[180:181], v[162:163], v[66:67] op_sel_hi:[0,1,1] neg_lo:[1,0,0] neg_hi:[1,0,0]\n\t"
    "v_pk_fma_f32 v[68:69], v[182:183], v[160:161], v[68:69] op_sel_hi:[0,1,1] neg_lo:[1,0,0] neg_hi:[1,0,0]\n\t"
    "v_pk_fma_f32 v[70:71], v[182:183], v[162:163], v[70:71] op_sel_hi:[0,1,1] neg_lo:[1,0,0] neg_hi:[1,0,0]\n\t"
    "v_cvt_pk_bf16_f32 v64, v64, v65\n\t"
    "v_cvt_pk_bf16_f32 v65, v66, v67\n\t"
    "v_cvt_pk_bf16_f32 v68, v68, v69\n\t"
    "v_cvt_pk_bf16_f32 v69, v70, v71\n\t"
    "v_pk_max_i16 v64, v64, 0\n\t"
    "v_pk_max_i16 v65, v65, 0\n\t"
    "v_pk_max_i16 v68, v68, 0\n\t"
    "v_pk_max_i16 v69, v69, 0\n\t"
    "ds_write_b64 %[Hw0], v[64:65] offset:2048\n\t"
    "ds_write_b64 %[Hw1], v[68:69] offset:2048\n\t"
    "buffer_load_dwordx4 a[108:111], %[voff], s[36:39], s56 offen offset:3072 sc1\n\t"
    "s_add_u32 s56, s56, 4096\n\t"
    "buffer_load_dwordx4 a[112:115], %[voff], s[36:39], s56 offen offset:0 sc1\n\t"
    "v_pk_fma_f32 v[72:73], v[184:185], v[160:161], v[72:73] op_sel_hi:[0,1,1] neg_lo:[1,0,0] neg_hi:[1,0,0]\n\t"
    "v_pk_fma_f32 v[74:75], v[184:185], v[162:163], v[74:75] op_sel_hi:[0,1,1] neg_lo:[1,0,0] neg_hi:[1,0,0]\n\t"
    "v_pk_fma_f32 v[76:77], v[186:187], v[160:161], v[76:77] op_sel_hi:[0,1,1] neg_lo:[1,0,0] neg_hi:[1,0,0]\n\t"
    "v_pk_fma_f32 v[78:79], v[186:187], v[162:163], v[78:79] op_sel_hi:[0,1,1] neg_lo:[1,0,0] neg_hi:[1,0,0]\n\t"
    "v_cvt_pk_bf16_f32 v72, v72, v73\n\t"
    "v_cvt_pk_bf16_f32 v73, v74, v75\n\t"
    "v_cvt_pk_bf16_f32 v76, v76, v77\n\t"
    "v_cvt_pk_bf16_f32 v77, v78, v79\n\t"
    "v_pk_max_i16 v72, v72, 0\n\t"
    "v_pk_max_i16 v73, v73, 0\n\t"
    "v_pk_max_i16 v76, v76, 0\n\t"
    "v_pk_max_i16 v77, v77, 0\n\t"
    "ds_write_b64 %[Hw2], v[72:73] offset:2048\n\t"
    "ds_write_b64 %[Hw3], v[76:77] offset:2048\n\t"
    "buffer_load_dwordx4 a[116:119], %[voff], s[36:39], s56 offen offset:1024 sc1\n\t"
    "v_pk_fma_f32 v[80:81], v[180:181], v[164:165], v[80:81] op_sel_hi:[0,1,1] neg_lo:[1,0,0] neg_hi:[1,0,0]\n\t"
    "v_pk_fma_f32 v[82:83], v[180:181], v[166:167], v[82:83] op_sel_hi:[0,1,1] neg_lo:[1,0,0] neg_hi:[1,0,0]\n\t"
    "v_pk_fma_f32 v[84:85], v[182:183], v[164:165], v[84:85] op_sel_hi:[0,1,1] neg_lo:[1,0,0] neg_hi:[1,0,0]\n\t"
    "v_pk_fma_f32 v[86:87], v[182:183], v[166:167], v[86:87] op_sel_hi:[0,1,1] neg_lo:[1,0,0] neg_hi:[1,0,0]\n\t"
    "v_cvt_pk_bf16_f32 v80, v80, v81\n\t"
    "v_cvt_pk_bf16_f32 v81, v82, v83\n\t"
    "v_cvt_pk_bf16_f32 v84, v84, v85\n\t"
    "v_cvt_pk_bf16_f32 v85, v86, v87\n\t"
    "v_pk_max_i16 v80, v80, 0\n\t"
    "v_pk_max_i16 v81, v81, 0\n\t"
    "v_pk_max_i16 v84, v84, 0\n\t"
    "v_pk_max_i16 v85, v85, 0\n\t"
    "ds_write_b64 %[Hw0], v[80:81] offset:2560\n\t"
    "ds_write_b64 %[Hw1], v[84:85] offset:2560\n\t"
    "buffer_load_dwordx4 a[120:123], %[voff], s[36:39], s56 offen offset:2048 sc1\n\t"
    "v_pk_fma_f32 v[88:89], v[184:185], v[164:165], v[88:89] op_sel_hi:[0,1,1] neg_lo:[1,0,0] neg_hi:[1,0,0]\n\t"
    "v_pk_fma_f32 v[90:91], v[184:185], v[166:167], v[90:91] op_sel_hi:[0,1,1] neg_lo:[1,0,0] neg_hi:[1,0,0]\n\t"
    "v_pk_fma_f32 v[92:93], v[186:187], v[164:165], v[92:93] op_sel_hi:[0,1,1] neg_lo:[1,0,0] neg_hi:[1,0,0]\n\t"
    "v_pk_fma_f32 v[94:95], v[186:187], v[166:167], v[94:95] op_sel_hi:[0,1,1] neg_lo:[1,0,0] neg_hi:[1,0,0]\n\t"
    "v_cvt_pk_bf16_f32 v88, v88, v89\n\t"
    "v_cvt_pk_bf16_f32 v89, v90, v91\n\t"
    "v_cvt_pk_bf16_f32 v92, v92, v93\n\t"
    "v_cvt_pk_bf16_f32 v93, v94, v95\n\t"
    "v_pk_max_i16 v88, v88, 0\n\t"
    "v_pk_max_i16 v89, v89, 0\n\t"
    "v_pk_max_i16 v92, v92, 0\n\t"
    "v_pk_max_i16 v93, v93, 0\n\t"
    "ds_write_b64 %[Hw2], v[88:89] offset:2560\n\t"
    "ds_write_b64 %[Hw3], v[92:93] offset:2560\n\t"
    "buffer_load_dwordx4 a[124:127], %[voff], s[36:39], s56 offen offset:3072 sc1\n\t"
    "s_add_u32 s56, s56, 4096\n\t"
    "v_pk_fma_f32 v[96:97], v[180:181], v[168:169], v[96:97] op_sel_hi:[0,1,1] neg_lo:[1,0,0] neg_hi:[1,0,0]\n\t"
    "v_pk_fma_f32 v[98:99], v[180:181], v[170:171], v[98:99] op_sel_hi:[0,1,1] neg_lo:[1,0,0] neg_hi:[1,0,0]\n\t"
    "v_pk_fma_f32 v[100:101], v[182:183], v[168:169], v[100:101] op_sel_hi:[0,1,1] neg_lo:[1,0,0] neg_hi:[1,0,0]\n\t"
    "v_pk_fma_f32 v[102:103], v[182:183], v[170:171], v[102:103] op_sel_hi:[0,1,1] neg_lo:[1,0,0] neg_hi:[1,0,0]\n\t"
    "v_cvt_pk_bf16_f32 v96, v96, v97\n\t"
    "v_cvt_pk_bf16_f32 v97, v98, v99\n\t"
    "v_cvt_pk_bf16_f32 v100, v100, v101\n\t"
    "v_cvt_pk_bf16_f32 v101, v102, v103\n\t"
    "v_pk_max_i16 v96, v96, 0\n\t"
    "v_pk_max_i16 v97, v97, 0\n\t"
    "v_pk_max_i16 v100, v100, 0\n\t"
    "v_pk_max_i16 v101, v101, 0\n\t"
    "ds_write_b64 %[Hw0], v[96:97] offset:3072\n\t"
    "ds_write_b64 %[Hw1], v[100:101] offset:3072\n\t"
    "buffer_load_dwordx4 a[128:131], %[voff], s[36:39], s56 offen offset:0 sc1\n\t"
    "buffer_load_dwordx4 a[132:135], %[voff], s[36:39], s56 offen offset:1024 sc1\n\t"
    "v_pk_fma_f32 v[104:105], v[184:185], v[168:169], v[104:105] op_sel_hi:[0,1,1] neg_lo:[1,0,0] neg_hi:[1,0,0]\n\t"
    "v_pk_fma_f32 v[106:107], v[184:185], v[170:171], v[106:107] op_sel_hi:[0,1,1] neg_lo:[1,0,0] neg_hi:[1,0,0]\n\t"
    "v_pk_fma_f32 v[108:109], v[186:187], v[168:169], v[108:109] op_sel_hi:[0,1,1] neg_lo:[1,0,0] neg_hi:[1,0,0]\n\t"
    "v_pk_fma_f32 v[110:111], v[186:187], v[170:171], v[110:111] op_sel_hi:[0,1,1] neg_lo:[1,0,0] neg_hi:[1,0,0]\n\t"
    "v_cvt_pk_bf16_f32 v104, v104, v105\n\t"
    "v_cvt_pk_bf16_f32 v105, v106, v107\n\t"
    "v_cvt_pk_bf16_f32 v108, v108, v109\n\t"
    "v_cvt_pk_bf16_f32 v109, v110, v111\n\t"
    "v_pk_max_i16 v104, v104, 0\n\t"
    "v_pk_max_i16 v105, v105, 0\n\t"
    "v_pk_max_i16 v108, v108, 0\n\t"
    "v_pk_max_i16 v109, v109, 0\n\t"
    "ds_write_b64 %[Hw2], v[104:105] offset:3072\n\t"
    "ds_write_b64 %[Hw3], v[108:109] offset:3072\n\t"
    "buffer_load_dwordx4 a[136:139], %[voff], s[36:39], s56 offen offset:2048 sc1\n\t"
    "v_pk_fma_f32 v[112:113], v[180:181], v[172:173], v[112:113] op_sel_hi:[0,1,1] neg_lo:[1,0,0] neg_hi:[1,0,0]\n\t"
    "v_pk_fma_f32 v[114:115], v[180:181], v[174:175], v[114:115] op_sel_hi:[0,1,1] neg_lo:[1,0,0] neg_hi:[1,0,0]\n\t"
    "v_pk_fma_f32 v[116:117], v[182:183], v[172:173], v[116:117] op_sel_hi:[0,1,1] neg_lo:[1,0,0] neg_hi:[1,0,0]\n\t"
    "v_pk_fma_f32 v[118:119], v[182:183], v[174:175], v[118:119] op_sel_hi:[0,1,1] neg_lo:[1,0,0] neg_hi:[1,0,0]\n\t"
    "v_cvt_pk_bf16_f32 v112, v112, v113\n\t"
    "v_cvt_pk_bf16_f32 v113, v114, v115\n\t"
    "v_cvt_pk_bf16_f32 v116, v116, v117\n\t"
    "v_cvt_pk_bf16_f32 v117, v118, v119\n\t"
    "v_pk_max_i16 v112, v112, 0\n\t"
    "v_pk_max_i16 v113, v113, 0\n\t"
    "v_pk_max_i16 v116, v116, 0\n\t"
    "v_pk_max_i16 v117, v117, 0\n\t"
    "ds_write_b64 %[Hw0], v[112:113] offset:3584\n\t"
    "ds_write_b64 %[Hw1], v[116:117] offset:3584\n\t"
    "buffer_load_dwordx4 a[140:143], %[voff], s[36:39], s56 offen offset:3072 sc1\n\t"
    "s_add_u32 s56, s56, 4096\n\t"
    "v_pk_fma_f32 v[120:121], v[184:185], v[172:173], v[120:121] op_sel_hi:[0,1,1] neg_lo:[1,0,0] neg_hi:[1,0,0]\n\t"
    "v_pk_fma_f32 v[122:123], v[184:185], v[174:175], v[122:123] op_sel_hi:[0,1,1] neg_lo:[1,0,0] neg_hi:[1,0,0]\n\t"
    "v_pk_fma_f32 v[124:125], v[186:187], v[172:173], v[124:125] op_sel_hi:[0,1,1] neg_lo:[1,0,0] neg_hi:[1,0,0]\n\t"
    "v_pk_fma_f32 v[126:127], v[186:187], v[174:175], v[126:127] op_sel_hi:[0,1,1] neg_lo:[1,0,0] neg_hi:[1,0,0]\n\t"
    "v_cvt_pk_bf16_f32 v120, v120, v121\n\t"
    "v_cvt_pk_bf16_f32 v121, v122, v123\n\t"
    "v_cvt_pk_bf16_f32 v124, v124, v125\n\t"
    "v_cvt_pk_bf16_f32 v125, v126, v127\n\t"
    "v_pk_max_i16 v120, v120, 0\n\t"
    "v_pk_max_i16 v121, v121, 0\n\t"
    "v_pk_max_i16 v124, v124, 0\n\t"
    "v_pk_max_i16 v125, v125, 0\n\t"
    "ds_write_b64 %[Hw2], v[120:121] offset:3584\n\t"
    "ds_write_b64 %[Hw3], v[124:125] offset:3584\n\t"
    "v_pk_fma_f32 v[128:129], v[180:181], v[176:177], v[128:129] op_sel_hi:[0,1,1] neg_lo:[1,0,0] neg_hi:[1,0,0]\n\t"
    "v_pk_fma_f32 v[130:131], v[180:181], v[178:179], v[130:131] op_sel_hi:[0,1,1] neg_lo:[1,0,0] neg_hi:[1,0,0]\n\t"
    "v_pk_fma_f32 v[132:133], v[182:183], v[176:177], v[132:133] op_sel_hi:[0,1,1] neg_lo:[1,0,0] neg_hi:[1,0,0]\n\t"
    "v_pk_fma_f32 v[134:135], v[182:183], v[178:179], v[134:135] op_sel_hi:[0,1,1] neg_lo:[1,0,0] neg_hi:[1,0,0]\n\t"
    "v_cvt_pk_bf16_f32 v128, v128, v129\n\t"
    "v_cvt_pk_bf16_f32 v129, v130, v131\n\t"
    "v_cvt_pk_bf16_f32 v132, v132, v133\n\t"
    "v_cvt_pk_bf16_f32 v133, v134, v135\n\t"
    "v_pk_max_i16 v128, v128, 0\n\t"
    "v_pk_max_i16 v129, v129, 0\n\t"
    "v_pk_max_i16 v132, v132, 0\n\t"
    "v_pk_max_i16 v133, v133, 0\n\t"
    "ds_write_b64 %[Hw0], v[128:129] offset:4096\n\t"
    "ds_write_b64 %[Hw1], v[132:133] offset:4096\n\t"
    "v_pk_fma_f32 v[136:137], v[184:185], v[176:177], v[136:137] op_sel_hi:[0,1,1] neg_lo:[1,0,0] neg_hi:[1,0,0]\n\t"
    "v_pk_fma_f32 v[138:139], v[184:185], v[178:179], v[138:139] op_sel_hi:[0,1,1] neg_lo:[1,0,0] neg_hi:[1,0,0]\n\t"
    "v_pk_fma_f32 v[140:141], v[186:187], v[176:177], v[140:141] op_sel_hi:[0,1,1] neg_lo:[1,0,0] neg_hi:[1,0,0]\n\t"
    "v_pk_fma_f32 v[142:143], v[186:187], v[178:179], v[142:143] op_sel_hi:[0,1,1] neg_lo:[1,0,0] neg_hi:[1,0,0]\n\t"
    "v_cvt_pk_bf16_f32 v136, v136, v137\n\t"
    "v_cvt_pk_bf16_f32 v137, v138, v139\n\t"
    "v_cvt_pk_bf16_f32 v140, v140, v141\n\t"
    "v_cvt_pk_bf16_f32 v141, v142, v143\n\t"
    "v_pk_max_i16 v136, v136, 0\n\t"
    "v_pk_max_i16 v137, v137, 0\n\t"
    "v_pk_max_i16 v140, v140, 0\n\t"
    "v_pk_max_i16 v141, v141, 0\n\t"
    "ds_write_b64 %[Hw2], v[136:137] offset:4096\n\t"
    "ds_write_b64 %[Hw3], v[140:141] offset:4096\n\t"
    "v_add_u32 v209, %[hrb], %[voff]\n\t"
    "v_add_u32 v208, 0x9000, v209\n\t"
    "v_mov_b32 v210, 1\n\t"
    "s_waitcnt lgkmcnt(0)\n\t"
    "s_barrier\n\t"
    "ds_read_b128 v[0:3], v209 offset:0\n\t"
    "ds_read_b128 v[4:7], v209 offset:18432\n\t"
    "ds_read_b128 v[8:11], v208 offset:0\n\t"
    "ds_read_b128 v[12:15], v208 offset:18432\n\t"
    "ds_read_b128 v[16:19], v209 offset:1024\n\t"
    "ds_read_b128 v[20:23], v209 offset:19456\n\t"
    "ds_read_b128 v[24:27], v208 offset:1024\n\t"
    "ds_read_b128 v[28:31], v208 offset:19456\n\t"
    "s_waitcnt vmcnt(35) lgkmcnt(4)\n\t"
    "v_mfma_f32_16x16x32_bf16 v[176:179], a[0:3], v[0:3], 0\n\t"
    "ds_read_b128 v[32:35], v209 offset:2048\n\t"
    "v_mfma_f32_16x16x32_bf16 v[180:183], a[0:3], v[4:7], 0\n\t"
    "ds_read_b128 v[36:39], v209 offset:20480\n\t"
    "v_mfma_f32_16x16x32_bf16 v[184:187], a[0:3], v[8:11], 0\n\t"
    "ds_read_b128 v[40:43], v208 offset:2048\n\t"
    "v_mfma_f32_16x16x32_bf16 v[188:191], a[0:3], v[12:15], 0\n\t"
    "ds_read_b128 v[44:47], v208 offset:20480\n\t"
    "s_waitcnt vmcnt(34) lgkmcnt(4)\n\t"
    "v_mfma_f32_16x16x32_bf16 v[176:179], a[4:7], v[16:19], v[176:179]\n\t"
    "ds_read_b128 v[48:51], v209 offset:3072\n\t"
    "buffer_load_dwordx4 a[0:3], %[voff], s[36:39], s56 offen offset:0 sc1\n\t"
    "v_mfma_f32_16x16x32_bf16 v[180:183], a[4:7], v[20:23], v[180:183]\n\t"
    "ds_read_b128 v[52:55], v209 offset:21504\n\t"
    "v_mfma_f32_16x16x32_bf16 v[184:187], a[4:7], v[24:27], v[184:187]\n\t"
    "ds_read_b128 v[56:59], v208 offset:3072\n\t"
    "v_mfma_f32_16x16x32_bf16 v[188:191], a[4:7], v[28:31], v[188:191]\n\t"
    "ds_read_b128 v[60:63], v208 offset:21504\n\t"
    "s_waitcnt vmcnt(34) lgkmcnt(4)\n\t"
    "v_mfma_f32_16x16x32_bf16 v[176:179], a[8:11], v[32:35], v[176:179]\n\t"
    "ds_read_b128 v[64:67], v209 offset:4096\n\t"
    "buffer_load_dwordx4 a[4:7], %[voff], s[36:39], s56 offen offset:1024 sc1\n\t"
    "v_mfma_f32_16x16x32_bf16 v[180:183], a[8:11], v[36:39], v[180:183]\n\t"
    "ds_read_b128 v[68:71], v209 offset:22528\n\t"
    "v_mfma_f32_16x16x32_bf16 v[184:187], a[8:11], v[40:43], v[184:187]\n\t"
    "ds_read_b128 v[72:75], v208 offset:4096\n\t"
    "v_mfma_f32_16x16x32_bf16 v[188:191], a[8:11], v[44:47], v[188:191]\n\t"
    "ds_read_b128 v[76:79], v208 offset:22528\n\t"
    "s_waitcnt vmcnt(34) lgkmcnt(4)\n\t"
    "v_mfma_f32_16x16x32_bf16 v[176:179], a[12:15], v[48:51], v[176:179]\n\t"
    "ds_read_b128 v[80:83], v209 offset:5120\n\t"
    "buffer_load_dwordx4 a[8:11], %[voff], s[36:39], s56 offen offset:2048 sc1\n\t"
    "v_mfma_f32_16x16x32_bf16 v[180:183], a[12:15], v[52:55], v[180:183]\n\t"
    "ds_read_b128 v[84:87], v209 offset:23552\n\t"
    "v_mfma_f32_16x16x32_bf16 v[184:187], a[12:15], v[56:59], v[184:187]\n\t"
    "ds_read_b128 v[88:91], v208 offset:5120\n\t"
    "v_mfma_f32_16x16x32_bf16 v[188:191], a[12:15], v[60:63], v[188:191]\n\t"
    "ds_read_b128 v[92:95], v208 offset:23552\n\t"
    "s_waitcnt vmcnt(34) lgkmcnt(4)\n\t"
    "v_mfma_f32_16x16x32_bf16 v[176:179], a[16:19], v[64:67], v[176:179]\n\t"
    "ds_read_b128 v[96:99], v209 offset:6144\n\t"
    "buffer_load_dwordx4 a[12:15], %[voff], s[36:39], s56 offen offset:3072 sc1\n\t"
    "v_mfma_f32_16x16x32_bf16 v[180:183], a[16:19], v[68:71], v[180:183]\n\t"
    "ds_read_b128 v[100:103], v209 offset:24576\n\t"
    "s_add_u32 s56, s56, 4096\n\t"
    "v_mfma_f32_16x16x32_bf16 v[184:187], a[16:19], v[72:75], v[184:187]\n\t"
    "ds_read_b128 v[104:107], v208 offset:6144\n\t"
    "v_mfma_f32_16x16x32_bf16 v[188:191], a[16:19], v[76:79], v[188:191]\n\t"
    "ds_read_b128 v[108:111], v208 offset:24576\n\t"
    "s_waitcnt vmcnt(34) lgkmcnt(4)\n\t"
    "v_mfma_f32_16x16x32_bf16 v[176:179], a[20:23], v[80:83], v[176:179]\n\t"
    "ds_read_b128 v[112:115], v209 offset:7168\n\t"
    "buffer_load_dwordx4 a[16:19], %[voff], s[36:39], s56 offen offset:0 sc1\n\t"
    "v_mfma_f32_16x16x32_bf16 v[180:183], a[20:23], v[84:87], v[180:183]\n\t"
    "ds_read_b128 v[116:119], v209 offset:25600\n\t"
    "v_mfma_f32_16x16x32_bf16 v[184:187], a[20:23], v[88:91], v[184:187]\n\t"
    "ds_read_b128 v[120:123], v208 offset:7168\n\t"
    "v_mfma_f32_16x16x32_bf16 v[188:191], a[20:23], v[92:95], v[188:191]\n\t"
    "ds_read_b128 v[124:127], v208 offset:25600\n\t"
    "s_waitcnt vmcnt(34) lgkmcnt(4)\n\t"
    "v_mfma_f32_16x16x32_bf16 v[176:179], a[24:27], v[96:99], v[176:179]\n\t"
    "ds_read_b128 v[128:131], v209 offset:8192\n\t"
    "buffer_load_dwordx4 a[20:23], %[voff], s[36:39], s56 offen offset:1024 sc1\n\t"
    "v_mfma_f32_16x16x32_bf16 v[180:183], a[24:27], v[100:103], v[180:183]\n\t"
    "ds_read_b128 v[132:135], v209 offset:26624\n\t"
    "v_mfma_f32_16x16x32_bf16 v[184:187], a[24:27], v[104:107], v[184:187]\n\t"
    "ds_read_b128 v[136:139], v208 offset:8192\n\t"
    "v_mfma_f32_16x16x32_bf16 v[188:191], a[24:27], v[108:111], v[188:191]\n\t"
    "ds_read_b128 v[140:143], v208 offset:26624\n\t"
    "s_waitcnt vmcnt(34) lgkmcnt(4)\n\t"
    "v_mfma_f32_16x16x32_bf16 v[176:179], a[28:31], v[112:115], v[176:179]\n\t"
    "ds_read_b128 v[144:147], v209 offset:9216\n\t"
    "buffer_load_dwordx4 a[24:27], %[voff], s[36:39], s56 offen offset:2048 sc1\n\t"
    "v_mfma_f32_16x16x32_bf16 v[180:183], a[28:31], v[116:119], v[180:183]\n\t"
    "ds_read_b128 v[148:151], v209 offset:27648\n\t"
    "v_mfma_f32_16x16x32_bf16 v[184:187], a[28:31], v[120:123], v[184:187]\n\t"
    "ds_read_b128 v[152:155], v208 offset:9216\n\t"
    "v_mfma_f32_16x16x32_bf16 v[188:191], a[28:31], v[124:127], v[188:191]\n\t"
    "ds_read_b128 v[156:159], v208 offset:27648\n\t"
    "s_waitcnt vmcnt(34) lgkmcnt(4)\n\t"
    "v_mfma_f32_16x16x32_bf16 v[176:179], a[32:35], v[128:131], v[176:179]\n\t"
    "ds_read_b128 v[160:163], v209 offset:10240\n\t"
    "buffer_load_dwordx4 a[28:31], %[voff], s[36:39], s56 offen offset:3072 sc1\n\t"
    "v_mfma_f32_16x16x32_bf16 v[180:183], a[32:35], v[132:135], v[180:183]\n\t"
    "ds_read_b128 v[164:167], v209 offset:28672\n\t"
    "s_add_u32 s56, s56, 4096\n\t"
    "v_mfma_f32_16x16x32_bf16 v[184:187], a[32:35], v[136:139], v[184:187]\n\t"
    "ds_read_b128 v[168:171], v208 offset:10240\n\t"
    "v_mfma_f32_16x16x32_bf16 v[188:191], a[32:35], v[140:143], v[188:191]\n\t"
    "ds_read_b128 v[172:175], v208 offset:28672\n\t"
    "s_waitcnt vmcnt(34) lgkmcnt(4)\n\t"
    "v_mfma_f32_16x16x32_bf16 v[176:179], a[36:39], v[144:147], v[176:179]\n\t"
    "ds_read_b128 a[144:147], v209 offset:11264\n\t"
    "buffer_load_dwordx4 a[32:35], %[voff], s[36:39], s56 offen offset:0 sc1\n\t"
    "v_mfma_f32_16x16x32_bf16 v[180:183], a[36:39], v[148:151], v[180:183]\n\t"
    "ds_read_b128 a[148:151], v209 offset:29696\n\t"
    "v_mfma_f32_16x16x32_bf16 v[184:187], a[36:39], v[152:155], v[184:187]\n\t"
    "ds_read_b128 a[152:155], v208 offset:11264\n\t"
    "v_mfma_f32_16x16x32_bf16 v[188:191], a[36:39], v[156:159], v[188:191]\n\t"
    "ds_read_b128 a[156:159], v208 offset:29696\n\t"
    "s_waitcnt vmcnt(34) lgkmcnt(4)\n\t"
    "v_mfma_f32_16x16x32_bf16 v[176:179], a[40:43], v[160:163], v[176:179]\n\t"
    "ds_read_b128 a[160:163], v209 offset:12288\n\t"
    "buffer_load_dwordx4 a[36:39], %[voff], s[36:39], s56 offen offset:1024 sc1\n\t"
    "v_mfma_f32_16x16x32_bf16 v[180:183], a[40:43], v[164:167], v[180:183]\n\t"
    "ds_read_b128 a[164:167], v209 offset:30720\n\t"
    "v_mfma_f32_16x16x32_bf16 v[184:187], a[40:43], v[168:171], v[184:187]\n\t"
    "ds_read_b128 a[168:171], v208 offset:12288\n\t"
    "v_mfma_f32_16x16x32_bf16 v[188:191], a[40:43], v[172:175], v[188:191]\n\t"
    "ds_read_b128 a[172:175], v208 offset:30720\n\t"
    "s_waitcnt vmcnt(34) lgkmcnt(4)\n\t"
    "v_mfma_f32_16x16x32_bf16 v[176:179], a[44:47], a[144:147], v[176:179]\n\t"
    "ds_read_b128 a[176:179], v209 offset:13312\n\t"
    "buffer_load_dwordx4 a[40:43], %[voff], s[36:39], s56 offen offset:2048 sc1\n\t"
    "v_mfma_f32_16x16x32_bf16 v[180:183], a[44:47], a[148:151], v[180:183]\n\t"
    "ds_read_b128 a[180:183], v209 offset:31744\n\t"
    "v_mfma_f32_16x16x32_bf16 v[184:187], a[44:47], a[152:155], v[184:187]\n\t"
    "ds_read_b128 a[184:187], v208 offset:13312\n\t"
    "v_mfma_f32_16x16x32_bf16 v[188:191], a[44:47], a[156:159], v[188:191]\n\t"
    "ds_read_b128 a[188:191], v208 offset:31744\n\t"
    "s_waitcnt vmcnt(34) lgkmcnt(4)\n\t"
    "v_mfma_f32_16x16x32_bf16 v[176:179], a[48:51], a[160:163], v[176:179]\n\t"
    "ds_read_b128 a[192:195], v209 offset:14336\n\t"
    "buffer_load_dwordx4 a[44:47], %[voff], s[36:39], s56 offen offset:3072 sc1\n\t"
    "v_mfma_f32_16x16x32_bf16 v[180:183], a[48:51], a[164:167], v[180:183]\n\t"
    "ds_read_b128 a[196:199], v209 offset:32768\n\t"
    "s_add_u32 s56, s56, 4096\n\t"
    "v_mfma_f32_16x16x32_bf16 v[184:187], a[48:51], a[168:171], v[184:187]\n\t"
    "ds_read_b128 a[200:203], v208 offset:14336\n\t"
    "v_mfma_f32_16x16x32_bf16 v[188:191], a[48:51], a[172:175], v[188:191]\n\t"
    "ds_read_b128 a[204:207], v208 offset:32768\n\t"
    "s_waitcnt vmcnt(34) lgkmcnt(4)\n\t"
    "v_mfma_f32_16x16x32_bf16 v[176:179], a[52:55], a[176:179], v[176:179]\n\t"
    "ds_read_b128 a[208:211], v209 offset:15360\n\t"
    "buffer_load_dwordx4 a[48:51], %[voff], s[36:39], s56 offen offset:0 sc1\n\t"
    "v_mfma_f32_16x16x32_bf16 v[180:183], a[52:55], a[180:183], v[180:183]\n\t"
    "ds_read_b128 a[212:215], v209 offset:33792\n\t"
    "v_mfma_f32_16x16x32_bf16 v[184:187], a[52:55], a[184:187], v[184:187]\n\t"
    "ds_read_b128 a[216:219], v208 offset:15360\n\t"
    "v_mfma_f32_16x16x32_bf16 v[188:191], a[52:55], a[188:191], v[188:191]\n\t"
    "ds_read_b128 a[220:223], v208 offset:33792\n\t"
    "s_waitcnt vmcnt(34) lgkmcnt(4)\n\t"
    "v_mfma_f32_16x16x32_bf16 v[176:179], a[56:59], a[192:195], v[176:179]\n\t"
    "ds_read_b128 a[224:227], v209 offset:16384\n\t"
    "buffer_load_dwordx4 a[52:55], %[voff], s[36:39], s56 offen offset:1024 sc1\n\t"
    "v_mfma_f32_16x16x32_bf16 v[180:183], a[56:59], a[196:199], v[180:183]\n\t"
    "ds_read_b128 a[228:231], v209 offset:34816\n\t"
    "v_mfma_f32_16x16x32_bf16 v[184:187], a[56:59], a[200:203], v[184:187]\n\t"
    "ds_read_b128 a[232:235], v208 offset:16384\n\t"
    "v_mfma_f32_16x16x32_bf16 v[188:191], a[56:59], a[204:207], v[188:191]\n\t"
    "ds_read_b128 a[236:239], v208 offset:34816\n\t"
    "s_waitcnt vmcnt(34) lgkmcnt(4)\n\t"
    "v_mfma_f32_16x16x32_bf16 v[176:179], a[60:63], a[208:211], v[176:179]\n\t"
    "ds_read_b128 a[240:243], v209 offset:17408\n\t"
    "buffer_load_dwordx4 a[56:59], %[voff], s[36:39], s56 offen offset:2048 sc1\n\t"
    "v_mfma_f32_16x16x32_bf16 v[180:183], a[60:63], a[212:215], v[180:183]\n\t"
    "ds_read_b128 a[244:247], v209 offset:35840\n\t"
    "v_mfma_f32_16x16x32_bf16 v[184:187], a[60:63], a[216:219], v[184:187]\n\t"
    "ds_read_b128 a[248:251], v208 offset:17408\n\t"
    "v_mfma_f32_16x16x32_bf16 v[188:191], a[60:63], a[220:223], v[188:191]\n\t"
    "ds_read_b128 a[252:255], v208 offset:35840\n\t"
    "s_waitcnt vmcnt(34) lgkmcnt(4)\n\t"
    "v_mfma_f32_16x16x32_bf16 v[176:179], a[64:67], a[224:227], v[176:179]\n\t"
    "buffer_load_dwordx4 a[60:63], %[voff], s[36:39], s56 offen offset:3072 sc1\n\t"
    "v_mfma_f32_16x16x32_bf16 v[180:183], a[64:67], a[228:231], v[180:183]\n\t"
    "s_add_u32 s56, s56, 4096\n\t"
    "v_mfma_f32_16x16x32_bf16 v[184:187], a[64:67], a[232:235], v[184:187]\n\t"
    "v_mfma_f32_16x16x32_bf16 v[188:191], a[64:67], a[236:239], v[188:191]\n\t"
    "s_waitcnt vmcnt(34) lgkmcnt(0)\n\t"
    "v_mfma_f32_16x16x32_bf16 v[176:179], a[68:71], a[240:243], v[176:179]\n\t"
    "buffer_load_dwordx4 a[64:67], %[voff], s[36:39], s56 offen offset:0 sc1\n\t"
    "v_mfma_f32_16x16x32_bf16 v[180:183], a[68:71], a[244:247], v[180:183]\n\t"
    "v_mfma_f32_16x16x32_bf16 v[184:187], a[68:71], a[248:251], v[184:187]\n\t"
    "v_mfma_f32_16x16x32_bf16 v[188:191], a[68:71], a[252:255], v[188:191]\n\t"
    "s_waitcnt vmcnt(34)\n\t"
    "v_mfma_f32_16x16x32_bf16 v[192:195], a[72:75], v[0:3], 0\n\t"
    "buffer_load_dwordx4 a[68:71], %[voff], s[36:39], s56 offen offset:1024 sc1\n\t"
    "v_mfma_f32_16x16x32_bf16 v[196:199], a[72:75], v[4:7], 0\n\t"
    "v_mfma_f32_16x16x32_bf16 v[200:203], a[72:75], v[8:11], 0\n\t"
    "v_mfma_f32_16x16x32_bf16 v[204:207], a[72:75], v[12:15], 0\n\t"
    "s_waitcnt vmcnt(34)\n\t"
    "v_mfma_f32_16x16x32_bf16 v[192:195], a[76:79], v[16:19], v[192:195]\n\t"
    "s_lshl_b32 s60, %[wid], 14\n\t"
    "s_lshl_b32 s61, %[fs], 10\n\t"
    "s_add_u32 s60, s60, s61\n\t"
    "s_add_u32 s57, s60, 7168\n\t"
    "buffer_load_dwordx4 a[72:75], %[voff], s[36:39], s56 offen offset:2048 sc1\n\t"
    "v_mfma_f32_16x16x32_bf16 v[196:199], a[76:79], v[20:23], v[196:199]\n\t"
    "v_mfma_f32_16x16x32_bf16 v[200:203], a[76:79], v[24:27], v[200:203]\n\t"
    "v_cvt_pk_bf16_f32 v176, v176, v177\n\t"
    "v_cvt_pk_bf16_f32 v177, v178, v179\n\t"
    "v_mfma_f32_16x16x32_bf16 v[204:207], a[76:79], v[28:31], v[204:207]\n\t"
    "v_cvt_pk_bf16_f32 v178, v180, v181\n\t"
    "v_cvt_pk_bf16_f32 v179, v182, v183\n\t"
    "s_waitcnt vmcnt(34)\n\t"
    "v_mfma_f32_16x16x32_bf16 v[192:195], a[80:83], v[32:35], v[192:195]\n\t"
    "v_cvt_pk_bf16_f32 v184, v184, v185\n\t"
    "v_cvt_pk_bf16_f32 v185, v186, v187\n\t"
    "buffer_load_dwordx4 a[76:79], %[voff], s[36:39], s56 offen offset:3072 sc1\n\t"
    "v_mfma_f32_16x16x32_bf16 v[196:199], a[80:83], v[36:39], v[196:199]\n\t"
    "v_cvt_pk_bf16_f32 v186, v188, v189\n\t"
    "v_cvt_pk_bf16_f32 v187, v190, v191\n\t"
    "v_mfma_f32_16x16x32_bf16 v[200:203], a[80:83], v[40:43], v[200:203]\n\t"
    "buffer_store_dwordx4 v[176:179], %[voff], s[44:47], s60 offen offset:0 sc1\n\t"
    "v_mfma_f32_16x16x32_bf16 v[204:207], a[80:83], v[44:47], v[204:207]\n\t"
    "s_add_u32 s56, s56, 4096\n\t"
    "s_waitcnt vmcnt(35)\n\t"
    "v_mfma_f32_16x16x32_bf16 v[192:195], a[84:87], v[48:51], v[192:195]\n\t"
    "buffer_load_dwordx4 a[80:83], %[voff], s[36:39], s56 offen offset:0 sc1\n\t"
    "v_mfma_f32_16x16x32_bf16 v[196:199], a[84:87], v[52:55], v[196:199]\n\t"
    "v_mfma_f32_16x16x32_bf16 v[200:203], a[84:87], v[56:59], v[200:203]\n\t"
    "v_mfma_f32_16x16x32_bf16 v[204:207], a[84:87], v[60:63], v[204:207]\n\t"
    "s_waitcnt vmcnt(35)\n\t"
    "v_mfma_f32_16x16x32_bf16 v[192:195], a[88:91], v[64:67], v[192:195]\n\t"
    "v_mfma_f32_16x16x32_bf16 v[196:199], a[88:91], v[68:71], v[196:199]\n\t"
    "v_mfma_f32_16x16x32_bf16 v[200:203], a[88:91], v[72:75], v[200:203]\n\t"
    "v_mfma_f32_16x16x32_bf16 v[204:207], a[88:91], v[76:79], v[204:207]\n\t"
    "s_waitcnt vmcnt(34)\n\t"
    "v_mfma_f32_16x16x32_bf16 v[192:195], a[92:95], v[80:83], v[192:195]\n\t"
    "buffer_load_dwordx4 a[84:87], %[voff], s[36:39], s56 offen offset:1024 sc1\n\t"
    "v_mfma_f32_16x16x32_bf16 v[196:199], a[92:95], v[84:87], v[196:199]\n\t"
    "v_mfma_f32_16x16x32_bf16 v[200:203], a[92:95], v[88:91], v[200:203]\n\t"
    "v_mfma_f32_16x16x32_bf16 v[204:207], a[92:95], v[92:95], v[204:207]\n\t"
    "s_waitcnt vmcnt(34)\n\t"
    "v_mfma_f32_16x16x32_bf16 v[192:195], a[96:99], v[96:99], v[192:195]\n\t"
    "buffer_load_dwordx4 a[88:91], %[voff], s[36:39], s56 offen offset:2048 sc1\n\t"
    "v_mfma_f32_16x16x32_bf16 v[196:199], a[96:99], v[100:103], v[196:199]\n\t"
    "v_mfma_f32_16x16x32_bf16 v[200:203], a[96:99], v[104:107], v[200:203]\n\t"
    "v_mfma_f32_16x16x32_bf16 v[204:207], a[96:99], v[108:111], v[204:207]\n\t"
    "s_waitcnt vmcnt(34)\n\t"
    "v_mfma_f32_16x16x32_bf16 v[192:195], a[100:103], v[112:115], v[192:195]\n\t"
    "buffer_load_dwordx4 a[92:95], %[voff], s[36:39], s56 offen offset:3072 sc1\n\t"
    "v_mfma_f32_16x16x32_bf16 v[196:199], a[100:103], v[116:119], v[196:199]\n\t"
    "s_add_u32 s56, s56, 4096\n\t"
    "v_mfma_f32_16x16x32_bf16 v[200:203], a[100:103], v[120:123], v[200:203]\n\t"
    "v_mfma_f32_16x16x32_bf16 v[204:207], a[100:103], v[124:127], v[204:207]\n\t"
    "s_waitcnt vmcnt(34)\n\t"
    "v_mfma_f32_16x16x32_bf16 v[192:195], a[104:107], v[128:131], v[192:195]\n\t"
    "buffer_load_dwordx4 a[96:99], %[voff], s[36:39], s56 offen offset:0 sc1\n\t"
    "v_mfma_f32_16x16x32_bf16 v[196:199], a[104:107], v[132:135], v[196:199]\n\t"
    "v_mfma_f32_16x16x32_bf16 v[200:203], a[104:107], v[136:139], v[200:203]\n\t"
    "v_mfma_f32_16x16x32_bf16 v[204:207], a[104:107], v[140:143], v[204:207]\n\t"
    "s_waitcnt vmcnt(34)\n\t"
    "v_mfma_f32_16x16x32_bf16 v[192:195], a[108:111], v[144:147], v[192:195]\n\t"
    "buffer_load_dwordx4 a[100:103], %[voff], s[36:39], s56 offen offset:1024 sc1\n\t"
    "v_mfma_f32_16x16x32_bf16 v[196:199], a[108:111], v[148:151], v[196:199]\n\t"
    "buffer_store_dwordx4 v[184:187], %[voff], s[44:47], s57 offen offset:1024 sc1\n\t"
    "v_mfma_f32_16x16x32_bf16 v[200:203], a[108:111], v[152:155], v[200:203]\n\t"
    "v_mfma_f32_16x16x32_bf16 v[204:207], a[108:111], v[156:159], v[204:207]\n\t"
    "s_waitcnt vmcnt(35)\n\t"
    "v_mfma_f32_16x16x32_bf16 v[192:195], a[112:115], v[160:163], v[192:195]\n\t"
    "buffer_load_dwordx4 a[104:107], %[voff], s[36:39], s56 offen offset:2048 sc1\n\t"
    "v_mfma_f32_16x16x32_bf16 v[196:199], a[112:115], v[164:167], v[196:199]\n\t"
    "v_mfma_f32_16x16x32_bf16 v[200:203], a[112:115], v[168:171], v[200:203]\n\t"
    "v_mfma_f32_16x16x32_bf16 v[204:207], a[112:115], v[172:175], v[204:207]\n\t"
    "s_waitcnt vmcnt(35)\n\t"
    "v_mfma_f32_16x16x32_bf16 v[192:195], a[116:119], a[144:147], v[192:195]\n\t"
    "buffer_load_dwordx4 a[108:111], %[voff], s[36:39], s56 offen offset:3072 sc1\n\t"
    "v_mfma_f32_16x16x32_bf16 v[196:199], a[116:119], a[148:151], v[196:199]\n\t"
    "s_add_u32 s56, s56, 4096\n\t"
    "v_mfma_f32_16x16x32_bf16 v[200:203], a[116:119], a[152:155], v[200:203]\n\t"
    "v_mfma_f32_16x16x32_bf16 v[204:207], a[116:119], a[156:159], v[204:207]\n\t"
    "s_waitcnt vmcnt(35)\n\t"
    "v_mfma_f32_16x16x32_bf16 v[192:195], a[120:123], a[160:163], v[192:195]\n\t"
    "buffer_load_dwordx4 a[112:115], %[voff], s[36:39], s56 offen offset:0 sc1\n\t"
    "v_mfma_f32_16x16x32_bf16 v[196:199], a[120:123], a[164:167], v[196:199]\n\t"
    "v_mfma_f32_16x16x32_bf16 v[200:203], a[120:123], a[168:171], v[200:203]\n\t"
    "v_mfma_f32_16x16x32_bf16 v[204:207], a[120:123], a[172:175], v[204:207]\n\t"
    "s_waitcnt vmcnt(35)\n\t"
    "v_mfma_f32_16x16x32_bf16 v[192:195], a[124:127], a[176:179], v[192:195]\n\t"
    "buffer_load_dwordx4 a[116:119], %[voff], s[36:39], s56 offen offset:1024 sc1\n\t"
    "v_mfma_f32_16x16x32_bf16 v[196:199], a[124:127], a[180:183], v[196:199]\n\t"
    "v_mfma_f32_16x16x32_bf16 v[200:203], a[124:127], a[184:187], v[200:203]\n\t"
    "v_mfma_f32_16x16x32_bf16 v[204:207], a[124:127], a[188:191], v[204:207]\n\t"
    "s_waitcnt vmcnt(35)\n\t"
    "v_mfma_f32_16x16x32_bf16 v[192:195], a[128:131], a[192:195], v[192:195]\n\t"
    "buffer_load_dwordx4 a[120:123], %[voff], s[36:39], s56 offen offset:2048 sc1\n\t"
    "v_mfma_f32_16x16x32_bf16 v[196:199], a[128:131], a[196:199], v[196:199]\n\t"
    "v_mfma_f32_16x16x32_bf16 v[200:203], a[128:131], a[200:203], v[200:203]\n\t"
    "v_mfma_f32_16x16x32_bf16 v[204:207], a[128:131], a[204:207], v[204:207]\n\t"
    "s_waitcnt vmcnt(35)\n\t"
    "v_mfma_f32_16x16x32_bf16 v[192:195], a[132:135], a[208:211], v[192:195]\n\t"
    "buffer_load_dwordx4 a[124:127], %[voff], s[36:39], s56 offen offset:3072 sc1\n\t"
    "v_mfma_f32_16x16x32_bf16 v[196:199], a[132:135], a[212:215], v[196:199]\n\t"
    "s_add_u32 s56, s56, 4096\n\t"
    "v_mfma_f32_16x16x32_bf16 v[200:203], a[132:135], a[216:219], v[200:203]\n\t"
    "v_mfma_f32_16x16x32_bf16 v[204:207], a[132:135], a[220:223], v[204:207]\n\t"
    "s_waitcnt vmcnt(35)\n\t"
    "v_mfma_f32_16x16x32_bf16 v[192:195], a[136:139], a[224:227], v[192:195]\n\t"
    "v_mfma_f32_16x16x32_bf16 v[196:199], a[136:139], a[228:231], v[196:199]\n\t"
    "v_mfma_f32_16x16x32_bf16 v[200:203], a[136:139], a[232:235], v[200:203]\n\t"
    "v_mfma_f32_16x16x32_bf16 v[204:207], a[136:139], a[236:239], v[204:207]\n\t"
    "s_waitcnt vmcnt(34)\n\t"
    "v_mfma_f32_16x16x32_bf16 v[192:195], a[140:143], a[240:243], v[192:195]\n\t"
    "buffer_load_dwordx4 a[128:131], %[voff], s[36:39], s56 offen offset:0 sc1\n\t"
    "v_mfma_f32_16x16x32_bf16 v[196:199], a[140:143], a[244:247], v[196:199]\n\t"
    "v_mfma_f32_16x16x32_bf16 v[200:203], a[140:143], a[248:251], v[200:203]\n\t"
    "v_mfma_f32_16x16x32_bf16 v[204:207], a[140:143], a[252:255], v[204:207]\n\t"
    "s_waitcnt vmcnt(34)\n\t"
    "v_mfma_f32_16x16x32_bf16 v[176:179], a[0:3], v[0:3], 0\n\t"
    "buffer_load_dwordx4 a[132:135], %[voff], s[36:39], s56 offen offset:1024 sc1\n\t"
    "v_mfma_f32_16x16x32_bf16 v[180:183], a[0:3], v[4:7], 0\n\t"
    "v_mfma_f32_16x16x32_bf16 v[184:187], a[0:3], v[8:11], 0\n\t"
    "v_mfma_f32_16x16x32_bf16 v[188:191], a[0:3], v[12:15], 0\n\t"
    "s_waitcnt vmcnt(34)\n\t"
    "v_mfma_f32_16x16x32_bf16 v[176:179], a[4:7], v[16:19], v[176:179]\n\t"
    "s_add_u32 s60, %[fs], 1\n\t"
    "s_and_b32 s60, s60, 7\n\t"
    "s_lshl_b32 s60, s60, 13\n\t"
    "s_add_u32 s60, s60, %[w2k]\n\t"
    "s_add_u32 s60, s60, 2097152\n\t"
    "buffer_load_dwordx4 a[136:139], %[voff], s[36:39], s56 offen offset:2048 sc1\n\t"
    "v_mfma_f32_16x16x32_bf16 v[180:183], a[4:7], v[20:23], v[180:183]\n\t"
    "v_mfma_f32_16x16x32_bf16 v[184:187], a[4:7], v[24:27], v[184:187]\n\t"
    "v_cvt_pk_bf16_f32 v192, v192, v193\n\t"
    "v_cvt_pk_bf16_f32 v193, v194, v195\n\t"
    "v_mfma_f32_16x16x32_bf16 v[188:191], a[4:7], v[28:31], v[188:191]\n\t"
    "v_cvt_pk_bf16_f32 v194, v196, v197\n\t"
    "v_cvt_pk_bf16_f32 v195, v198, v199\n\t"
    "s_waitcnt vmcnt(34)\n\t"
    "v_mfma_f32_16x16x32_bf16 v[176:179], a[8:11], v[32:35], v[176:179]\n\t"
    "v_cvt_pk_bf16_f32 v200, v200, v201\n\t"
    "v_cvt_pk_bf16_f32 v201, v202, v203\n\t"
    "buffer_load_dwordx4 a[140:143], %[voff], s[36:39], s56 offen offset:3072 sc1\n\t"
    "v_mfma_f32_16x16x32_bf16 v[180:183], a[8:11], v[36:39], v[180:183]\n\t"
    "v_cvt_pk_bf16_f32 v202, v204, v205\n\t"
    "v_cvt_pk_bf16_f32 v203, v206, v207\n\t"
    "v_mfma_f32_16x16x32_bf16 v[184:187], a[8:11], v[40:43], v[184:187]\n\t"
    "buffer_store_dwordx4 v[192:195], %[voff], s[44:47], s60 offen offset:0 sc1\n\t"
    "v_mfma_f32_16x16x32_bf16 v[188:191], a[8:11], v[44:47], v[188:191]\n\t"
    "s_add_u32 s56, s56, 4096\n\t"
    "s_waitcnt vmcnt(35)\n\t"
    "v_mfma_f32_16x16x32_bf16 v[176:179], a[12:15], v[48:51], v[176:179]\n\t"
    "buffer_load_dwordx4 a[0:3], %[voff], s[36:39], s56 offen offset:0 sc1\n\t"
    "v_mfma_f32_16x16x32_bf16 v[180:183], a[12:15], v[52:55], v[180:183]\n\t"
    "v_mfma_f32_16x16x32_bf16 v[184:187], a[12:15], v[56:59], v[184:187]\n\t"
    "v_mfma_f32_16x16x32_bf16 v[188:191], a[12:15], v[60:63], v[188:191]\n\t"
    "s_waitcnt vmcnt(35)\n\t"
    "v_mfma_f32_16x16x32_bf16 v[176:179], a[16:19], v[64:67], v[176:179]\n\t"
    "buffer_load_dwordx4 a[4:7], %[voff], s[36:39], s56 offen offset:1024 sc1\n\t"
    "v_mfma_f32_16x16x32_bf16 v[180:183], a[16:19], v[68:71], v[180:183]\n\t"
    "v_mfma_f32_16x16x32_bf16 v[184:187], a[16:19], v[72:75], v[184:187]\n\t"
    "v_mfma_f32_16x16x32_bf16 v[188:191], a[16:19], v[76:79], v[188:191]\n\t"
    "s_waitcnt vmcnt(35)\n\t"
    "v_mfma_f32_16x16x32_bf16 v[176:179], a[20:23], v[80:83], v[176:179]\n\t"
    "buffer_load_dwordx4 a[8:11], %[voff], s[36:39], s56 offen offset:2048 sc1\n\t"
    "v_mfma_f32_16x16x32_bf16 v[180:183], a[20:23], v[84:87], v[180:183]\n\t"
    "v_mfma_f32_16x16x32_bf16 v[184:187], a[20:23], v[88:91], v[184:187]\n\t"
    "v_mfma_f32_16x16x32_bf16 v[188:191], a[20:23], v[92:95], v[188:191]\n\t"
    "s_waitcnt vmcnt(35)\n\t"
    "v_mfma_f32_16x16x32_bf16 v[176:179], a[24:27], v[96:99], v[176:179]\n\t"
    "buffer_load_dwordx4 a[12:15], %[voff], s[36:39], s56 offen offset:3072 sc1\n\t"
    "v_mfma_f32_16x16x32_bf16 v[180:183], a[24:27], v[100:103], v[180:183]\n\t"
    "s_waitcnt vmcnt(15)\n\t"
    "s_mov_b64 exec, 0xff\n\t"
    "s_lshl_b32 s61, %[wid], 8\n\t"
    "v_lshlrev_b32 v211, 1, %[voff]\n\t"
    "buffer_atomic_add v210, v211, s[52:55], s61 offen\n\t"
    "s_mov_b64 exec, -1\n\t"
    "v_mfma_f32_16x16x32_bf16 v[184:187], a[24:27], v[104:107], v[184:187]\n\t"
    "s_add_u32 s56, s56, 4096\n\t"
    "v_mfma_f32_16x16x32_bf16 v[188:191], a[24:27], v[108:111], v[188:191]\n\t"
    "s_waitcnt vmcnt(36)\n\t"
    "v_mfma_f32_16x16x32_bf16 v[176:179], a[28:31], v[112:115], v[176:179]\n\t"
    "buffer_load_dwordx4 a[16:19], %[voff], s[36:39], s56 offen offset:0 sc1\n\t"
    "v_mfma_f32_16x16x32_bf16 v[180:183], a[28:31], v[116:119], v[180:183]\n\t"
    "v_mfma_f32_16x16x32_bf16 v[184:187], a[28:31], v[120:123], v[184:187]\n\t"
    "v_mfma_f32_16x16x32_bf16 v[188:191], a[28:31], v[124:127], v[188:191]\n\t"
    "s_waitcnt vmcnt(36)\n\t"
    "v_mfma_f32_16x16x32_bf16 v[176:179], a[32:35], v[128:131], v[176:179]\n\t"
    "buffer_load_dwordx4 a[20:23], %[voff], s[36:39], s56 offen offset:1024 sc1\n\t"
    "v_mfma_f32_16x16x32_bf16 v[180:183], a[32:35], v[132:135], v[180:183]\n\t"
    "v_mfma_f32_16x16x32_bf16 v[184:187], a[32:35], v[136:139], v[184:187]\n\t"
    "v_mfma_f32_16x16x32_bf16 v[188:191], a[32:35], v[140:143], v[188:191]\n\t"
    "s_waitcnt vmcnt(36)\n\t"
    "v_mfma_f32_16x16x32_bf16 v[176:179], a[36:39], v[144:147], v[176:179]\n\t"
    "v_mfma_f32_16x16x32_bf16 v[180:183], a[36:39], v[148:151], v[180:183]\n\t"
    "buffer_store_dwordx4 v[200:203], %[voff], s[44:47], s60 offen offset:1024 sc1\n\t"
    "v_mfma_f32_16x16x32_bf16 v[184:187], a[36:39], v[152:155], v[184:187]\n\t"
    "v_mfma_f32_16x16x32_bf16 v[188:191], a[36:39], v[156:159], v[188:191]\n\t"
    "s_waitcnt vmcnt(36)\n\t"
    "v_mfma_f32_16x16x32_bf16 v[176:179], a[40:43], v[160:163], v[176:179]\n\t"
    "buffer_load_dwordx4 a[24:27], %[voff], s[36:39], s56 offen offset:2048 sc1\n\t"
    "v_mfma_f32_16x16x32_bf16 v[180:183], a[40:43], v[164:167], v[180:183]\n\t"
    "v_mfma_f32_16x16x32_bf16 v[184:187], a[40:43], v[168:171], v[184:187]\n\t"
    "v_mfma_f32_16x16x32_bf16 v[188:191], a[40:43], v[172:175], v[188:191]\n\t"
    "s_waitcnt vmcnt(36)\n\t"
    "v_mfma_f32_16x16x32_bf16 v[176:179], a[44:47], a[144:147], v[176:179]\n\t"
    "buffer_load_dwordx4 a[28:31], %[voff], s[36:39], s56 offen offset:3072 sc1\n\t"
    "v_mfma_f32_16x16x32_bf16 v[180:183], a[44:47], a[148:151], v[180:183]\n\t"
    "s_add_u32 s56, s56, 4096\n\t"
    "v_mfma_f32_16x16x32_bf16 v[184:187], a[44:47], a[152:155], v[184:187]\n\t"
    "v_mfma_f32_16x16x32_bf16 v[188:191], a[44:47], a[156:159], v[188:191]\n\t"
    "s_waitcnt vmcnt(36)\n\t"
    "v_mfma_f32_16x16x32_bf16 v[176:179], a[48:51], a[160:163], v[176:179]\n\t"
    "buffer_load_dwordx4 a[32:35], %[voff], s[36:39], s56 offen offset:0 sc1\n\t"
    "v_mfma_f32_16x16x32_bf16 v[180:183], a[48:51], a[164:167], v[180:183]\n\t"
    "v_mfma_f32_16x16x32_bf16 v[184:187], a[48:51], a[168:171], v[184:187]\n\t"
    "v_mfma_f32_16x16x32_bf16 v[188:191], a[48:51], a[172:175], v[188:191]\n\t"
    "s_waitcnt vmcnt(36)\n\t"
    "v_mfma_f32_16x16x32_bf16 v[176:179], a[52:55], a[176:179], v[176:179]\n\t"
    "buffer_load_dwordx4 a[36:39], %[voff], s[36:39], s56 offen offset:1024 sc1\n\t"
    "v_mfma_f32_16x16x32_bf16 v[180:183], a[52:55], a[180:183], v[180:183]\n\t"
    "v_mfma_f32_16x16x32_bf16 v[184:187], a[52:55], a[184:187], v[184:187]\n\t"
    "v_mfma_f32_16x16x32_bf16 v[188:191], a[52:55], a[188:191], v[188:191]\n\t"
    "s_waitcnt vmcnt(36)\n\t"
    "v_mfma_f32_16x16x32_bf16 v[176:179], a[56:59], a[192:195], v[176:179]\n\t"
    "buffer_load_dwordx4 a[40:43], %[voff], s[36:39], s56 offen offset:2048 sc1\n\t"
    "v_mfma_f32_16x16x32_bf16 v[180:183], a[56:59], a[196:199], v[180:183]\n\t"
    "v_mfma_f32_16x16x32_bf16 v[184:187], a[56:59], a[200:203], v[184:187]\n\t"
    "v_mfma_f32_16x16x32_bf16 v[188:191], a[56:59], a[204:207], v[188:191]\n\t"
    "s_waitcnt vmcnt(36)\n\t"
    "v_mfma_f32_16x16x32_bf16 v[176:179], a[60:63], a[208:211], v[176:179]\n\t"
    "buffer_load_dwordx4 a[44:47], %[voff], s[36:39], s56 offen offset:3072 sc1\n\t"
    "v_mfma_f32_16x16x32_bf16 v[180:183], a[60:63], a[212:215], v[180:183]\n\t"
    "s_add_u32 s56, s56, 4096\n\t"
    "v_mfma_f32_16x16x32_bf16 v[184:187], a[60:63], a[216:219], v[184:187]\n\t"
    "v_mfma_f32_16x16x32_bf16 v[188:191], a[60:63], a[220:223], v[188:191]\n\t"
    "s_waitcnt vmcnt(36)\n\t"
    "v_mfma_f32_16x16x32_bf16 v[176:179], a[64:67], a[224:227], v[176:179]\n\t"
    "buffer_load_dwordx4 a[48:51], %[voff], s[36:39], s56 offen offset:0 sc1\n\t"
    "v_mfma_f32_16x16x32_bf16 v[180:183], a[64:67], a[228:231], v[180:183]\n\t"
    "v_mfma_f32_16x16x32_bf16 v[184:187], a[64:67], a[232:235], v[184:187]\n\t"
    "v_mfma_f32_16x16x32_bf16 v[188:191], a[64:67], a[236:239], v[188:191]\n\t"
    "s_waitcnt vmcnt(36)\n\t"
    "v_mfma_f32_16x16x32_bf16 v[176:179], a[68:71], a[240:243], v[176:179]\n\t"
    "buffer_load_dwordx4 a[52:55], %[voff], s[36:39], s56 offen offset:1024 sc1\n\t"
    "v_mfma_f32_16x16x32_bf16 v[180:183], a[68:71], a[244:247], v[180:183]\n\t"
    "v_mfma_f32_16x16x32_bf16 v[184:187], a[68:71], a[248:251], v[184:187]\n\t"
    "v_mfma_f32_16x16x32_bf16 v[188:191], a[68:71], a[252:255], v[188:191]\n\t"
    "s_waitcnt vmcnt(36)\n\t"
    "v_mfma_f32_16x16x32_bf16 v[192:195], a[72:75], v[0:3], 0\n\t"
    "buffer_load_dwordx4 a[56:59], %[voff], s[36:39], s56 offen offset:2048 sc1\n\t"
    "v_mfma_f32_16x16x32_bf16 v[196:199], a[72:75], v[4:7], 0\n\t"
    "v_mfma_f32_16x16x32_bf16 v[200:203], a[72:75], v[8:11], 0\n\t"
    "v_mfma_f32_16x16x32_bf16 v[204:207], a[72:75], v[12:15], 0\n\t"
    "s_waitcnt vmcnt(36)\n\t"
    "v_mfma_f32_16x16x32_bf16 v[192:195], a[76:79], v[16:19], v[192:195]\n\t"
    "s_add_u32 s60, %[fs], 2\n\t"
    "s_and_b32 s60, s60, 7\n\t"
    "s_lshl_b32 s60, s60, 13\n\t"
    "s_add_u32 s60, s60, %[w2k]\n\t"
    "s_add_u32 s60, s60, 4194304\n\t"
    "buffer_load_dwordx4 a[60:63], %[voff], s[36:39], s56 offen offset:3072 sc1\n\t"
    "v_mfma_f32_16x16x32_bf16 v[196:199], a[76:79], v[20:23], v[196:199]\n\t"
    "s_add_u32 s56, s56, 4096\n\t"
    "v_mfma_f32_16x16x32_bf16 v[200:203], a[76:79], v[24:27], v[200:203]\n\t"
    "v_cvt_pk_bf16_f32 v176, v176, v177\n\t"
    "v_cvt_pk_bf16_f32 v177, v178, v179\n\t"
    "v_mfma_f32_16x16x32_bf16 v[204:207], a[76:79], v[28:31], v[204:207]\n\t"
    "v_cvt_pk_bf16_f32 v178, v180, v181\n\t"
    "v_cvt_pk_bf16_f32 v179, v182, v183\n\t"
    "s_waitcnt vmcnt(35)\n\t"
    "v_mfma_f32_16x16x32_bf16 v[192:195], a[80:83], v[32:35], v[192:195]\n\t"
    "v_cvt_pk_bf16_f32 v184, v184, v185\n\t"
    "v_cvt_pk_bf16_f32 v185, v186, v187\n\t"
    "buffer_load_dwordx4 a[64:67], %[voff], s[36:39], s56 offen offset:0 sc1\n\t"
    "v_mfma_f32_16x16x32_bf16 v[196:199], a[80:83], v[36:39], v[196:199]\n\t"
    "v_cvt_pk_bf16_f32 v186, v188, v189\n\t"
    "v_cvt_pk_bf16_f32 v187, v190, v191\n\t"
    "v_mfma_f32_16x16x32_bf16 v[200:203], a[80:83], v[40:43], v[200:203]\n\t"
    "buffer_store_dwordx4 v[176:179], %[voff], s[44:47], s60 offen offset:0 sc1\n\t"
    "v_mfma_f32_16x16x32_bf16 v[204:207], a[80:83], v[44:47], v[204:207]\n\t"
    "s_waitcnt vmcnt(36)\n\t"
    "v_mfma_f32_16x16x32_bf16 v[192:195], a[84:87], v[48:51], v[192:195]\n\t"
    "v_mfma_f32_16x16x32_bf16 v[196:199], a[84:87], v[52:55], v[196:199]\n\t"
    "v_mfma_f32_16x16x32_bf16 v[200:203], a[84:87], v[56:59], v[200:203]\n\t"
    "v_mfma_f32_16x16x32_bf16 v[204:207], a[84:87], v[60:63], v[204:207]\n\t"
    "s_waitcnt vmcnt(35)\n\t"
    "v_mfma_f32_16x16x32_bf16 v[192:195], a[88:91], v[64:67], v[192:195]\n\t"
    "buffer_load_dwordx4 a[68:71], %[voff], s[36:39], s56 offen offset:1024 sc1\n\t"
    "v_mfma_f32_16x16x32_bf16 v[196:199], a[88:91], v[68:71], v[196:199]\n\t"
    "v_mfma_f32_16x16x32_bf16 v[200:203], a[88:91], v[72:75], v[200:203]\n\t"
    "v_mfma_f32_16x16x32_bf16 v[204:207], a[88:91], v[76:79], v[204:207]\n\t"
    "s_waitcnt vmcnt(35)\n\t"
    "v_mfma_f32_16x16x32_bf16 v[192:195], a[92:95], v[80:83], v[192:195]\n\t"
    "buffer_load_dwordx4 a[72:75], %[voff], s[36:39], s56 offen offset:2048 sc1\n\t"
    "v_mfma_f32_16x16x32_bf16 v[196:199], a[92:95], v[84:87], v[196:199]\n\t"
    "v_mfma_f32_16x16x32_bf16 v[200:203], a[92:95], v[88:91], v[200:203]\n\t"
    "v_mfma_f32_16x16x32_bf16 v[204:207], a[92:95], v[92:95], v[204:207]\n\t"
    "s_waitcnt vmcnt(35)\n\t"
    "v_mfma_f32_16x16x32_bf16 v[192:195], a[96:99], v[96:99], v[192:195]\n\t"
    "buffer_load_dwordx4 a[76:79], %[voff], s[36:39], s56 offen offset:3072 sc1\n\t"
    "v_mfma_f32_16x16x32_bf16 v[196:199], a[96:99], v[100:103], v[196:199]\n\t"
    "s_waitcnt vmcnt(15)\n\t"
    "s_mov_b64 exec, 1\n\t"
    "s_add_u32 s61, %[fs], 1\n\t"
    "s_and_b32 s61, s61, 7\n\t"
    "s_lshl_b32 s61, s61, 7\n\t"
    "s_add_u32 s61, s61, %[w32]\n\t"
    "buffer_atomic_add v210, off, s[52:55], s61 offset:0\n\t"
    "s_mov_b64 exec, -1\n\t"
    "v_mfma_f32_16x16x32_bf16 v[200:203], a[96:99], v[104:107], v[200:203]\n\t"
    "s_add_u32 s56, s56, 4096\n\t"
    "v_mfma_f32_16x16x32_bf16 v[204:207], a[96:99], v[108:111], v[204:207]\n\t"
    "s_waitcnt vmcnt(36)\n\t"
    "v_mfma_f32_16x16x32_bf16 v[192:195], a[100:103], v[112:115], v[192:195]\n\t"
    "buffer_load_dwordx4 a[80:83], %[voff], s[36:39], s56 offen offset:0 sc1\n\t"
    "v_mfma_f32_16x16x32_bf16 v[196:199], a[100:103], v[116:119], v[196:199]\n\t"
    "v_mfma_f32_16x16x32_bf16 v[200:203], a[100:103], v[120:123], v[200:203]\n\t"
    "v_mfma_f32_16x16x32_bf16 v[204:207], a[100:103], v[124:127], v[204:207]\n\t"
    "s_waitcnt vmcnt(35)\n\t"
    "v_mfma_f32_16x16x32_bf16 v[192:195], a[104:107], v[128:131], v[192:195]\n\t"
    "buffer_load_dwordx4 a[84:87], %[voff], s[36:39], s56 offen offset:1024 sc1\n\t"
    "v_mfma_f32_16x16x32_bf16 v[196:199], a[104:107], v[132:135], v[196:199]\n\t"
    "v_mfma_f32_16x16x32_bf16 v[200:203], a[104:107], v[136:139], v[200:203]\n\t"
    "v_mfma_f32_16x16x32_bf16 v[204:207], a[104:107], v[140:143], v[204:207]\n\t"
    "s_waitcnt vmcnt(35)\n\t"
    "v_mfma_f32_16x16x32_bf16 v[192:195], a[108:111], v[144:147], v[192:195]\n\t"
    "buffer_load_dwordx4 a[88:91], %[voff], s[36:39], s56 offen offset:2048 sc1\n\t"
    "v_mfma_f32_16x16x32_bf16 v[196:199], a[108:111], v[148:151], v[196:199]\n\t"
    "buffer_store_dwordx4 v[184:187], %[voff], s[44:47], s60 offen offset:1024 sc1\n\t"
    "v_mfma_f32_16x16x32_bf16 v[200:203], a[108:111], v[152:155], v[200:203]\n\t"
    "v_mfma_f32_16x16x32_bf16 v[204:207], a[108:111], v[156:159], v[204:207]\n\t"
    "s_waitcnt vmcnt(36)\n\t"
    "v_mfma_f32_16x16x32_bf16 v[192:195], a[112:115], v[160:163], v[192:195]\n\t"
    "buffer_load_dwordx4 a[92:95], %[voff], s[36:39], s56 offen offset:3072 sc1\n\t"
    "v_mfma_f32_16x16x32_bf16 v[196:199], a[112:115], v[164:167], v[196:199]\n\t"
    "s_add_u32 s56, s56, 4096\n\t"
    "v_mfma_f32_16x16x32_bf16 v[200:203], a[112:115], v[168:171], v[200:203]\n\t"
    "v_mfma_f32_16x16x32_bf16 v[204:207], a[112:115], v[172:175], v[204:207]\n\t"
    "s_waitcnt vmcnt(36)\n\t"
    "v_mfma_f32_16x16x32_bf16 v[192:195], a[116:119], a[144:147], v[192:195]\n\t"
    "buffer_load_dwordx4 a[96:99], %[voff], s[36:39], s56 offen offset:0 sc1\n\t"
    "v_mfma_f32_16x16x32_bf16 v[196:199], a[116:119], a[148:151], v[196:199]\n\t"
    "v_mfma_f32_16x16x32_bf16 v[200:203], a[116:119], a[152:155], v[200:203]\n\t"
    "v_mfma_f32_16x16x32_bf16 v[204:207], a[116:119], a[156:159], v[204:207]\n\t"
    "s_waitcnt vmcnt(36)\n\t"
    "v_mfma_f32_16x16x32_bf16 v[192:195], a[120:123], a[160:163], v[192:195]\n\t"
    "buffer_load_dwordx4 a[100:103], %[voff], s[36:39], s56 offen offset:1024 sc1\n\t"
    "v_mfma_f32_16x16x32_bf16 v[196:199], a[120:123], a[164:167], v[196:199]\n\t"
    "v_mfma_f32_16x16x32_bf16 v[200:203], a[120:123], a[168:171], v[200:203]\n\t"
    "v_mfma_f32_16x16x32_bf16 v[204:207], a[120:123], a[172:175], v[204:207]\n\t"
    "s_waitcnt vmcnt(36)\n\t"
    "v_mfma_f32_16x16x32_bf16 v[192:195], a[124:127], a[176:179], v[192:195]\n\t"
    "buffer_load_dwordx4 a[104:107], %[voff], s[36:39], s56 offen offset:2048 sc1\n\t"
    "v_mfma_f32_16x16x32_bf16 v[196:199], a[124:127], a[180:183], v[196:199]\n\t"
    "v_mfma_f32_16x16x32_bf16 v[200:203], a[124:127], a[184:187], v[200:203]\n\t"
    "v_mfma_f32_16x16x32_bf16 v[204:207], a[124:127], a[188:191], v[204:207]\n\t"
    "s_waitcnt vmcnt(36)\n\t"
    "v_mfma_f32_16x16x32_bf16 v[192:195], a[128:131], a[192:195], v[192:195]\n\t"
    "v_mfma_f32_16x16x32_bf16 v[196:199], a[128:131], a[196:199], v[196:199]\n\t"
    "v_mfma_f32_16x16x32_bf16 v[200:203], a[128:131], a[200:203], v[200:203]\n\t"
    "v_mfma_f32_16x16x32_bf16 v[204:207], a[128:131], a[204:207], v[204:207]\n\t"
    "s_waitcnt vmcnt(35)\n\t"
    "v_mfma_f32_16x16x32_bf16 v[192:195], a[132:135], a[208:211], v[192:195]\n\t"
    "buffer_load_dwordx4 a[108:111], %[voff], s[36:39], s56 offen offset:3072 sc1\n\t"
    "v_mfma_f32_16x16x32_bf16 v[196:199], a[132:135], a[212:215], v[196:199]\n\t"
    "s_add_u32 s56, s56, 4096\n\t"
    "v_mfma_f32_16x16x32_bf16 v[200:203], a[132:135], a[216:219], v[200:203]\n\t"
    "v_mfma_f32_16x16x32_bf16 v[204:207], a[132:135], a[220:223], v[204:207]\n\t"
    "s_waitcnt vmcnt(35)\n\t"
    "v_mfma_f32_16x16x32_bf16 v[192:195], a[136:139], a[224:227], v[192:195]\n\t"
    "buffer_load_dwordx4 a[112:115], %[voff], s[36:39], s56 offen offset:0 sc1\n\t"
    "v_mfma_f32_16x16x32_bf16 v[196:199], a[136:139], a[228:231], v[196:199]\n\t"
    "v_mfma_f32_16x16x32_bf16 v[200:203], a[136:139], a[232:235], v[200:203]\n\t"
    "v_mfma_f32_16x16x32_bf16 v[204:207], a[136:139], a[236:239], v[204:207]\n\t"
    "s_waitcnt vmcnt(35)\n\t"
    "v_mfma_f32_16x16x32_bf16 v[192:195], a[140:143], a[240:243], v[192:195]\n\t"
    "buffer_load_dwordx4 a[116:119], %[voff], s[36:39], s56 offen offset:1024 sc1\n\t"
    "v_mfma_f32_16x16x32_bf16 v[196:199], a[140:143], a[244:247], v[196:199]\n\t"
    "v_mfma_f32_16x16x32_bf16 v[200:203], a[140:143], a[248:251], v[200:203]\n\t"
    "v_mfma_f32_16x16x32_bf16 v[204:207], a[140:143], a[252:255], v[204:207]\n\t"
    "s_waitcnt vmcnt(34)\n\t"
    "v_mfma_f32_16x16x32_bf16 v[176:179], a[0:3], v[0:3], 0\n\t"
    "buffer_load_dwordx4 a[120:123], %[voff], s[36:39], s56 offen offset:2048 sc1\n\t"
    "v_mfma_f32_16x16x32_bf16 v[180:183], a[0:3], v[4:7], 0\n\t"
    "v_mfma_f32_16x16x32_bf16 v[184:187], a[0:3], v[8:11], 0\n\t"
    "v_mfma_f32_16x16x32_bf16 v[188:191], a[0:3], v[12:15], 0\n\t"
    "s_waitcnt vmcnt(34)\n\t"
    "v_mfma_f32_16x16x32_bf16 v[176:179], a[4:7], v[16:19], v[176:179]\n\t"
    "s_add_u32 s60, %[fs], 3\n\t"
    "s_and_b32 s60, s60, 7\n\t"
    "s_lshl_b32 s60, s60, 13\n\t"
    "s_add_u32 s60, s60, %[w2k]\n\t"
    "s_add_u32 s60, s60, 6291456\n\t"
    "buffer_load_dwordx4 a[124:127], %[voff], s[36:39], s56 offen offset:3072 sc1\n\t"
    "v_mfma_f32_16x16x32_bf16 v[180:183], a[4:7], v[20:23], v[180:183]\n\t"
    "s_add_u32 s56, s56, 4096\n\t"
    "v_mfma_f32_16x16x32_bf16 v[184:187], a[4:7], v[24:27], v[184:187]\n\t"
    "v_cvt_pk_bf16_f32 v192, v192, v193\n\t"
    "v_cvt_pk_bf16_f32 v193, v194, v195\n\t"
    "v_mfma_f32_16x16x32_bf16 v[188:191], a[4:7], v[28:31], v[188:191]\n\t"
    "v_cvt_pk_bf16_f32 v194, v196, v197\n\t"
    "v_cvt_pk_bf16_f32 v195, v198, v199\n\t"
    "s_waitcnt vmcnt(34)\n\t"
    "v_mfma_f32_16x16x32_bf16 v[176:179], a[8:11], v[32:35], v[176:179]\n\t"
    "v_cvt_pk_bf16_f32 v200, v200, v201\n\t"
    "v_cvt_pk_bf16_f32 v201, v202, v203\n\t"
    "buffer_load_dwordx4 a[128:131], %[voff], s[36:39], s56 offen offset:0 sc1\n\t"
    "v_mfma_f32_16x16x32_bf16 v[180:183], a[8:11], v[36:39], v[180:183]\n\t"
    "v_cvt_pk_bf16_f32 v202, v204, v205\n\t"
    "v_cvt_pk_bf16_f32 v203, v206, v207\n\t"
    "v_mfma_f32_16x16x32_bf16 v[184:187], a[8:11], v[40:43], v[184:187]\n\t"
    "buffer_store_dwordx4 v[192:195], %[voff], s[44:47], s60 offen offset:0 sc1\n\t"
    "v_mfma_f32_16x16x32_bf16 v[188:191], a[8:11], v[44:47], v[188:191]\n\t"
    "s_waitcnt vmcnt(35)\n\t"
    "v_mfma_f32_16x16x32_bf16 v[176:179], a[12:15], v[48:51], v[176:179]\n\t"
    "buffer_load_dwordx4 a[132:135], %[voff], s[36:39], s56 offen offset:1024 sc1\n\t"
    "v_mfma_f32_16x16x32_bf16 v[180:183], a[12:15], v[52:55], v[180:183]\n\t"
    "v_mfma_f32_16x16x32_bf16 v[184:187], a[12:15], v[56:59], v[184:187]\n\t"
    "v_mfma_f32_16x16x32_bf16 v[188:191], a[12:15], v[60:63], v[188:191]\n\t"
    "s_waitcnt vmcnt(34)\n\t"
    "v_mfma_f32_16x16x32_bf16 v[176:179], a[16:19], v[64:67], v[176:179]\n\t"
    "buffer_load_dwordx4 a[136:139], %[voff], s[36:39], s56 offen offset:2048 sc1\n\t"
    "v_mfma_f32_16x16x32_bf16 v[180:183], a[16:19], v[68:71], v[180:183]\n\t"
    "v_mfma_f32_16x16x32_bf16 v[184:187], a[16:19], v[72:75], v[184:187]\n\t"
    "v_mfma_f32_16x16x32_bf16 v[188:191], a[16:19], v[76:79], v[188:191]\n\t"
    "s_waitcnt vmcnt(34)\n\t"
    "v_mfma_f32_16x16x32_bf16 v[176:179], a[20:23], v[80:83], v[176:179]\n\t"
    "buffer_load_dwordx4 a[140:143], %[voff], s[36:39], s56 offen offset:3072 sc1\n\t"
    "v_mfma_f32_16x16x32_bf16 v[180:183], a[20:23], v[84:87], v[180:183]\n\t"
    "s_add_u32 s56, s56, 4096\n\t"
    "v_mfma_f32_16x16x32_bf16 v[184:187], a[20:23], v[88:91], v[184:187]\n\t"
    "v_mfma_f32_16x16x32_bf16 v[188:191], a[20:23], v[92:95], v[188:191]\n\t"
    "s_waitcnt vmcnt(33)\n\t"
    "v_mfma_f32_16x16x32_bf16 v[176:179], a[24:27], v[96:99], v[176:179]\n\t"
    "buffer_load_dwordx4 a[0:3], %[voff], s[36:39], s56 offen offset:0 sc1\n\t"
    "v_mfma_f32_16x16x32_bf16 v[180:183], a[24:27], v[100:103], v[180:183]\n\t"
    "s_waitcnt vmcnt(15)\n\t"
    "s_mov_b64 exec, 1\n\t"
    "s_add_u32 s61, %[fs], 2\n\t"
    "s_and_b32 s61, s61, 7\n\t"
    "s_lshl_b32 s61, s61, 7\n\t"
    "s_add_u32 s61, s61, %[w32]\n\t"
    "buffer_atomic_add v210, off, s[52:55], s61 offset:0\n\t"
    "s_mov_b64 exec, -1\n\t"
    "v_mfma_f32_16x16x32_bf16 v[184:187], a[24:27], v[104:107], v[184:187]\n\t"
    "v_mfma_f32_16x16x32_bf16 v[188:191], a[24:27], v[108:111], v[188:191]\n\t"
    "s_waitcnt vmcnt(34)\n\t"
    "v_mfma_f32_16x16x32_bf16 v[176:179], a[28:31], v[112:115], v[176:179]\n\t"
    "buffer_load_dwordx4 a[4:7], %[voff], s[36:39], s56 offen offset:1024 sc1\n\t"
    "v_mfma_f32_16x16x32_bf16 v[180:183], a[28:31], v[116:119], v[180:183]\n\t"
    "v_mfma_f32_16x16x32_bf16 v[184:187], a[28:31], v[120:123], v[184:187]\n\t"
    "v_mfma_f32_16x16x32_bf16 v[188:191], a[28:31], v[124:127], v[188:191]\n\t"
    "s_waitcnt vmcnt(34)\n\t"
    "v_mfma_f32_16x16x32_bf16 v[176:179], a[32:35], v[128:131], v[176:179]\n\t"
    "v_mfma_f32_16x16x32_bf16 v[180:183], a[32:35], v[132:135], v[180:183]\n\t"
    "v_mfma_f32_16x16x32_bf16 v[184:187], a[32:35], v[136:139], v[184:187]\n\t"
    "v_mfma_f32_16x16x32_bf16 v[188:191], a[32:35], v[140:143], v[188:191]\n\t"
    "s_waitcnt vmcnt(33)\n\t"
    "v_mfma_f32_16x16x32_bf16 v[176:179], a[36:39], v[144:147], v[176:179]\n\t"
    "buffer_load_dwordx4 a[8:11], %[voff], s[36:39], s56 offen offset:2048 sc1\n\t"
    "v_mfma_f32_16x16x32_bf16 v[180:183], a[36:39], v[148:151], v[180:183]\n\t"
    "buffer_store_dwordx4 v[200:203], %[voff], s[44:47], s60 offen offset:1024 sc1\n\t"
    "v_mfma_f32_16x16x32_bf16 v[184:187], a[36:39], v[152:155], v[184:187]\n\t"
    "v_mfma_f32_16x16x32_bf16 v[188:191], a[36:39], v[156:159], v[188:191]\n\t"
    "s_waitcnt vmcnt(34)\n\t"
    "v_mfma_f32_16x16x32_bf16 v[176:179], a[40:43], v[160:163], v[176:179]\n\t"
    "buffer_load_dwordx4 a[12:15], %[voff], s[36:39], s56 offen offset:3072 sc1\n\t"
    "v_mfma_f32_16x16x32_bf16 v[180:183], a[40:43], v[164:167], v[180:183]\n\t"
    "s_add_u32 s56, s56, 4096\n\t"
    "v_mfma_f32_16x16x32_bf16 v[184:187], a[40:43], v[168:171], v[184:187]\n\t"
    "v_mfma_f32_16x16x32_bf16 v[188:191], a[40:43], v[172:175], v[188:191]\n\t"
    "s_waitcnt vmcnt(34)\n\t"
    "v_mfma_f32_16x16x32_bf16 v[176:179], a[44:47], a[144:147], v[176:179]\n\t"
    "buffer_load_dwordx4 a[16:19], %[voff], s[36:39], s56 offen offset:0 sc1\n\t"
    "v_mfma_f32_16x16x32_bf16 v[180:183], a[44:47], a[148:151], v[180:183]\n\t"
    "v_mfma_f32_16x16x32_bf16 v[184:187], a[44:47], a[152:155], v[184:187]\n\t"
    "v_mfma_f32_16x16x32_bf16 v[188:191], a[44:47], a[156:159], v[188:191]\n\t"
    "s_waitcnt vmcnt(34)\n\t"
    "v_mfma_f32_16x16x32_bf16 v[176:179], a[48:51], a[160:163], v[176:179]\n\t"
    "buffer_load_dwordx4 a[20:23], %[voff], s[36:39], s56 offen offset:1024 sc1\n\t"
    "v_mfma_f32_16x16x32_bf16 v[180:183], a[48:51], a[164:167], v[180:183]\n\t"
    "v_mfma_f32_16x16x32_bf16 v[184:187], a[48:51], a[168:171], v[184:187]\n\t"
    "v_mfma_f32_16x16x32_bf16 v[188:191], a[48:51], a[172:175], v[188:191]\n\t"
    "s_waitcnt vmcnt(34)\n\t"
    "v_mfma_f32_16x16x32_bf16 v[176:179], a[52:55], a[176:179], v[176:179]\n\t"
    "buffer_load_dwordx4 a[24:27], %[voff], s[36:39], s56 offen offset:2048 sc1\n\t"
    "v_mfma_f32_16x16x32_bf16 v[180:183], a[52:55], a[180:183], v[180:183]\n\t"
    "v_mfma_f32_16x16x32_bf16 v[184:187], a[52:55], a[184:187], v[184:187]\n\t"
    "v_mfma_f32_16x16x32_bf16 v[188:191], a[52:55], a[188:191], v[188:191]\n\t"
    "s_waitcnt vmcnt(34)\n\t"
    "v_mfma_f32_16x16x32_bf16 v[176:179], a[56:59], a[192:195], v[176:179]\n\t"
    "buffer_load_dwordx4 a[28:31], %[voff], s[36:39], s56 offen offset:3072 sc1\n\t"
    "v_mfma_f32_16x16x32_bf16 v[180:183], a[56:59], a[196:199], v[180:183]\n\t"
    "s_add_u32 s56, s56, 4096\n\t"
    "v_mfma_f32_16x16x32_bf16 v[184:187], a[56:59], a[200:203], v[184:187]\n\t"
    "v_mfma_f32_16x16x32_bf16 v[188:191], a[56:59], a[204:207], v[188:191]\n\t"
    "s_waitcnt vmcnt(34)\n\t"
    "v_mfma_f32_16x16x32_bf16 v[176:179], a[60:63], a[208:211], v[176:179]\n\t"
    "buffer_load_dwordx4 a[32:35], %[voff], s[36:39], s56 offen offset:0 sc1\n\t"
    "v_mfma_f32_16x16x32_bf16 v[180:183], a[60:63], a[212:215], v[180:183]\n\t"
    "v_mfma_f32_16x16x32_bf16 v[184:187], a[60:63], a[216:219], v[184:187]\n\t"
    "v_mfma_f32_16x16x32_bf16 v[188:191], a[60:63], a[220:223], v[188:191]\n\t"
    "s_waitcnt vmcnt(34)\n\t"
    "v_mfma_f32_16x16x32_bf16 v[176:179], a[64:67], a[224:227], v[176:179]\n\t"
    "buffer_load_dwordx4 a[36:39], %[voff], s[36:39], s56 offen offset:1024 sc1\n\t"
    "v_mfma_f32_16x16x32_bf16 v[180:183], a[64:67], a[228:231], v[180:183]\n\t"
    "v_mfma_f32_16x16x32_bf16 v[184:187], a[64:67], a[232:235], v[184:187]\n\t"
    "v_mfma_f32_16x16x32_bf16 v[188:191], a[64:67], a[236:239], v[188:191]\n\t"
    "s_waitcnt vmcnt(33)\n\t"
    "v_mfma_f32_16x16x32_bf16 v[176:179], a[68:71], a[240:243], v[176:179]\n\t"
    "buffer_load_dwordx4 a[40:43], %[voff], s[36:39], s56 offen offset:2048 sc1\n\t"
    "v_mfma_f32_16x16x32_bf16 v[180:183], a[68:71], a[244:247], v[180:183]\n\t"
    "v_mfma_f32_16x16x32_bf16 v[184:187], a[68:71], a[248:251], v[184:187]\n\t"
    "v_mfma_f32_16x16x32_bf16 v[188:191], a[68:71], a[252:255], v[188:191]\n\t"
    "s_waitcnt vmcnt(33)\n\t"
    "v_mfma_f32_16x16x32_bf16 v[192:195], a[72:75], v[0:3], 0\n\t"
    "buffer_load_dwordx4 a[44:47], %[voff], s[36:39], s56 offen offset:3072 sc1\n\t"
    "v_mfma_f32_16x16x32_bf16 v[196:199], a[72:75], v[4:7], 0\n\t"
    "s_add_u32 s56, s56, 4096\n\t"
    "v_mfma_f32_16x16x32_bf16 v[200:203], a[72:75], v[8:11], 0\n\t"
    "v_mfma_f32_16x16x32_bf16 v[204:207], a[72:75], v[12:15], 0\n\t"
    "s_waitcnt vmcnt(33)\n\t"
    "v_mfma_f32_16x16x32_bf16 v[192:195], a[76:79], v[16:19], v[192:195]\n\t"
    "s_add_u32 s60, %[fs], 4\n\t"
    "s_and_b32 s60, s60, 7\n\t"
    "s_lshl_b32 s60, s60, 13\n\t"
    "s_add_u32 s60, s60, %[w2k]\n\t"
    "s_add_u32 s60, s60, 8388608\n\t"
    "v_mfma_f32_16x16x32_bf16 v[196:199], a[76:79], v[20:23], v[196:199]\n\t"
    "v_mfma_f32_16x16x32_bf16 v[200:203], a[76:79], v[24:27], v[200:203]\n\t"
    "v_cvt_pk_bf16_f32 v176, v176, v177\n\t"
    "v_cvt_pk_bf16_f32 v177, v178, v179\n\t"
    "v_mfma_f32_16x16x32_bf16 v[204:207], a[76:79], v[28:31], v[204:207]\n\t"
    "v_cvt_pk_bf16_f32 v178, v180, v181\n\t"
    "v_cvt_pk_bf16_f32 v179, v182, v183\n\t"
    "s_waitcnt vmcnt(31)\n\t"
    "v_mfma_f32_16x16x32_bf16 v[192:195], a[80:83], v[32:35], v[192:195]\n\t"
    "v_cvt_pk_bf16_f32 v184, v184, v185\n\t"
    "v_cvt_pk_bf16_f32 v185, v186, v187\n\t"
    "buffer_load_dwordx4 a[48:51], %[voff], s[36:39], s56 offen offset:0 sc1\n\t"
    "v_mfma_f32_16x16x32_bf16 v[196:199], a[80:83], v[36:39], v[196:199]\n\t"
    "v_cvt_pk_bf16_f32 v186, v188, v189\n\t"
    "v_cvt_pk_bf16_f32 v187, v190, v191\n\t"
    "v_mfma_f32_16x16x32_bf16 v[200:203], a[80:83], v[40:43], v[200:203]\n\t"
    "buffer_store_dwordx4 v[176:179], %[voff], s[44:47], s60 offen offset:0 sc1\n\t"
    "v_mfma_f32_16x16x32_bf16 v[204:207], a[80:83], v[44:47], v[204:207]\n\t"
    "s_waitcnt vmcnt(32)\n\t"
    "v_mfma_f32_16x16x32_bf16 v[192:195], a[84:87], v[48:51], v[192:195]\n\t"
    "buffer_load_dwordx4 a[52:55], %[voff], s[36:39], s56 offen offset:1024 sc1\n\t"
    "v_mfma_f32_16x16x32_bf16 v[196:199], a[84:87], v[52:55], v[196:199]\n\t"
    "v_mfma_f32_16x16x32_bf16 v[200:203], a[84:87], v[56:59], v[200:203]\n\t"
    "v_mfma_f32_16x16x32_bf16 v[204:207], a[84:87], v[60:63], v[204:207]\n\t"
    "s_waitcnt vmcnt(32)\n\t"
    "v_mfma_f32_16x16x32_bf16 v[192:195], a[88:91], v[64:67], v[192:195]\n\t"
    "buffer_load_dwordx4 a[56:59], %[voff], s[36:39], s56 offen offset:2048 sc1\n\t"
    "v_mfma_f32_16x16x32_bf16 v[196:199], a[88:91], v[68:71], v[196:199]\n\t"
    "v_mfma_f32_16x16x32_bf16 v[200:203], a[88:91], v[72:75], v[200:203]\n\t"
    "v_mfma_f32_16x16x32_bf16 v[204:207], a[88:91], v[76:79], v[204:207]\n\t"
    "s_waitcnt vmcnt(31)\n\t"
    "v_mfma_f32_16x16x32_bf16 v[192:195], a[92:95], v[80:83], v[192:195]\n\t"
    "buffer_load_dwordx4 a[60:63], %[voff], s[36:39], s56 offen offset:3072 sc1\n\t"
    "v_mfma_f32_16x16x32_bf16 v[196:199], a[92:95], v[84:87], v[196:199]\n\t"
    "s_add_u32 s56, s56, 4096\n\t"
    "v_mfma_f32_16x16x32_bf16 v[200:203], a[92:95], v[88:91], v[200:203]\n\t"
    "v_mfma_f32_16x16x32_bf16 v[204:207], a[92:95], v[92:95], v[204:207]\n\t"
    "s_waitcnt vmcnt(31)\n\t"
    "v_mfma_f32_16x16x32_bf16 v[192:195], a[96:99], v[96:99], v[192:195]\n\t"
    "buffer_load_dwordx4 a[64:67], %[voff], s[36:39], s56 offen offset:0 sc1\n\t"
    "v_mfma_f32_16x16x32_bf16 v[196:199], a[96:99], v[100:103], v[196:199]\n\t"
    "s_waitcnt vmcnt(15)\n\t"
    "s_mov_b64 exec, 1\n\t"
    "s_add_u32 s61, %[fs], 3\n\t"
    "s_and_b32 s61, s61, 7\n\t"
    "s_lshl_b32 s61, s61, 7\n\t"
    "s_add_u32 s61, s61, %[w32]\n\t"
    "buffer_atomic_add v210, off, s[52:55], s61 offset:0\n\t"
    "s_mov_b64 exec, -1\n\t"
    "v_mfma_f32_16x16x32_bf16 v[200:203], a[96:99], v[104:107], v[200:203]\n\t"
    "v_mfma_f32_16x16x32_bf16 v[204:207], a[96:99], v[108:111], v[204:207]\n\t"
    "s_waitcnt vmcnt(32)\n\t"
    "v_mfma_f32_16x16x32_bf16 v[192:195], a[100:103], v[112:115], v[192:195]\n\t"
    "buffer_load_dwordx4 a[68:71], %[voff], s[36:39], s56 offen offset:1024 sc1\n\t"
    "v_mfma_f32_16x16x32_bf16 v[196:199], a[100:103], v[116:119], v[196:199]\n\t"
    "v_mfma_f32_16x16x32_bf16 v[200:203], a[100:103], v[120:123], v[200:203]\n\t"
    "v_mfma_f32_16x16x32_bf16 v[204:207], a[100:103], v[124:127], v[204:207]\n\t"
    "s_waitcnt vmcnt(32)\n\t"
    "v_mfma_f32_16x16x32_bf16 v[192:195], a[104:107], v[128:131], v[192:195]\n\t"
    "buffer_load_dwordx4 a[72:75], %[voff], s[36:39], s56 offen offset:2048 sc1\n\t"
    "v_mfma_f32_16x16x32_bf16 v[196:199], a[104:107], v[132:135], v[196:199]\n\t"
    "v_mfma_f32_16x16x32_bf16 v[200:203], a[104:107], v[136:139], v[200:203]\n\t"
    "v_mfma_f32_16x16x32_bf16 v[204:207], a[104:107], v[140:143], v[204:207]\n\t"
    "s_waitcnt vmcnt(32)\n\t"
    "v_mfma_f32_16x16x32_bf16 v[192:195], a[108:111], v[144:147], v[192:195]\n\t"
    "buffer_load_dwordx4 a[76:79], %[voff], s[36:39], s56 offen offset:3072 sc1\n\t"
    "v_mfma_f32_16x16x32_bf16 v[196:199], a[108:111], v[148:151], v[196:199]\n\t"
    "buffer_store_dwordx4 v[184:187], %[voff], s[44:47], s60 offen offset:1024 sc1\n\t"
    "v_mfma_f32_16x16x32_bf16 v[200:203], a[108:111], v[152:155], v[200:203]\n\t"
    "s_add_u32 s56, s56, 4096\n\t"
    "v_mfma_f32_16x16x32_bf16 v[204:207], a[108:111], v[156:159], v[204:207]\n\t"
    "s_waitcnt vmcnt(33)\n\t"
    "v_mfma_f32_16x16x32_bf16 v[192:195], a[112:115], v[160:163], v[192:195]\n\t"
    "buffer_load_dwordx4 a[80:83], %[voff], s[36:39], s56 offen offset:0 sc1\n\t"
    "v_mfma_f32_16x16x32_bf16 v[196:199], a[112:115], v[164:167], v[196:199]\n\t"
    "v_mfma_f32_16x16x32_bf16 v[200:203], a[112:115], v[168:171], v[200:203]\n\t"
    "v_mfma_f32_16x16x32_bf16 v[204:207], a[112:115], v[172:175], v[204:207]\n\t"
    "s_waitcnt vmcnt(33)\n\t"
    "v_mfma_f32_16x16x32_bf16 v[192:195], a[116:119], a[144:147], v[192:195]\n\t"
    "buffer_load_dwordx4 a[84:87], %[voff], s[36:39], s56 offen offset:1024 sc1\n\t"
    "v_mfma_f32_16x16x32_bf16 v[196:199], a[116:119], a[148:151], v[196:199]\n\t"
    "v_mfma_f32_16x16x32_bf16 v[200:203], a[116:119], a[152:155], v[200:203]\n\t"
    "v_mfma_f32_16x16x32_bf16 v[204:207], a[116:119], a[156:159], v[204:207]\n\t"
    "s_waitcnt vmcnt(33)\n\t"
    "v_mfma_f32_16x16x32_bf16 v[192:195], a[120:123], a[160:163], v[192:195]\n\t"
    "buffer_load_dwordx4 a[88:91], %[voff], s[36:39], s56 offen offset:2048 sc1\n\t"
    "v_mfma_f32_16x16x32_bf16 v[196:199], a[120:123], a[164:167], v[196:199]\n\t"
    "v_mfma_f32_16x16x32_bf16 v[200:203], a[120:123], a[168:171], v[200:203]\n\t"
    "v_mfma_f32_16x16x32_bf16 v[204:207], a[120:123], a[172:175], v[204:207]\n\t"
    "s_waitcnt vmcnt(33)\n\t"
    "v_mfma_f32_16x16x32_bf16 v[192:195], a[124:127], a[176:179], v[192:195]\n\t"
    "v_mfma_f32_16x16x32_bf16 v[196:199], a[124:127], a[180:183], v[196:199]\n\t"
    "v_mfma_f32_16x16x32_bf16 v[200:203], a[124:127], a[184:187], v[200:203]\n\t"
    "v_mfma_f32_16x16x32_bf16 v[204:207], a[124:127], a[188:191], v[204:207]\n\t"
    "s_waitcnt vmcnt(32)\n\t"
    "v_mfma_f32_16x16x32_bf16 v[192:195], a[128:131], a[192:195], v[192:195]\n\t"
    "buffer_load_dwordx4 a[92:95], %[voff], s[36:39], s56 offen offset:3072 sc1\n\t"
    "v_mfma_f32_16x16x32_bf16 v[196:199], a[128:131], a[196:199], v[196:199]\n\t"
    "s_add_u32 s56, s56, 4096\n\t"
    "v_mfma_f32_16x16x32_bf16 v[200:203], a[128:131], a[200:203], v[200:203]\n\t"
    "v_mfma_f32_16x16x32_bf16 v[204:207], a[128:131], a[204:207], v[204:207]\n\t"
    "s_waitcnt vmcnt(31)\n\t"
    "v_mfma_f32_16x16x32_bf16 v[192:195], a[132:135], a[208:211], v[192:195]\n\t"
    "buffer_load_dwordx4 a[96:99], %[voff], s[36:39], s56 offen offset:0 sc1\n\t"
    "v_mfma_f32_16x16x32_bf16 v[196:199], a[132:135], a[212:215], v[196:199]\n\t"
    "v_mfma_f32_16x16x32_bf16 v[200:203], a[132:135], a[216:219], v[200:203]\n\t"
    "v_mfma_f32_16x16x32_bf16 v[204:207], a[132:135], a[220:223], v[204:207]\n\t"
    "s_waitcnt vmcnt(31)\n\t"
    "v_mfma_f32_16x16x32_bf16 v[192:195], a[136:139], a[224:227], v[192:195]\n\t"
    "buffer_load_dwordx4 a[100:103], %[voff], s[36:39], s56 offen offset:1024 sc1\n\t"
    "v_mfma_f32_16x16x32_bf16 v[196:199], a[136:139], a[228:231], v[196:199]\n\t"
    "v_mfma_f32_16x16x32_bf16 v[200:203], a[136:139], a[232:235], v[200:203]\n\t"
    "v_mfma_f32_16x16x32_bf16 v[204:207], a[136:139], a[236:239], v[204:207]\n\t"
    "s_waitcnt vmcnt(31)\n\t"
    "v_mfma_f32_16x16x32_bf16 v[192:195], a[140:143], a[240:243], v[192:195]\n\t"
    "buffer_load_dwordx4 a[104:107], %[voff], s[36:39], s56 offen offset:2048 sc1\n\t"
    "v_mfma_f32_16x16x32_bf16 v[196:199], a[140:143], a[244:247], v[196:199]\n\t"
    "v_mfma_f32_16x16x32_bf16 v[200:203], a[140:143], a[248:251], v[200:203]\n\t"
    "v_mfma_f32_16x16x32_bf16 v[204:207], a[140:143], a[252:255], v[204:207]\n\t"
    "s_waitcnt vmcnt(31)\n\t"
    "v_mfma_f32_16x16x32_bf16 v[176:179], a[0:3], v[0:3], 0\n\t"
    "buffer_load_dwordx4 a[108:111], %[voff], s[36:39], s56 offen offset:3072 sc1\n\t"
    "v_mfma_f32_16x16x32_bf16 v[180:183], a[0:3], v[4:7], 0\n\t"
    "s_add_u32 s56, s56, 4096\n\t"
    "v_mfma_f32_16x16x32_bf16 v[184:187], a[0:3], v[8:11], 0\n\t"
    "v_mfma_f32_16x16x32_bf16 v[188:191], a[0:3], v[12:15], 0\n\t"
    "s_waitcnt vmcnt(30)\n\t"
    "v_mfma_f32_16x16x32_bf16 v[176:179], a[4:7], v[16:19], v[176:179]\n\t"
    "s_add_u32 s60, %[fs], 5\n\t"
    "s_and_b32 s60, s60, 7\n\t"
    "s_lshl_b32 s60, s60, 13\n\t"
    "s_add_u32 s60, s60, %[w2k]\n\t"
    "s_add_u32 s60, s60, 10485760\n\t"
    "buffer_load_dwordx4 a[112:115], %[voff], s[36:39], s56 offen offset:0 sc1\n\t"
    "v_mfma_f32_16x16x32_bf16 v[180:183], a[4:7], v[20:23], v[180:183]\n\t"
    "v_mfma_f32_16x16x32_bf16 v[184:187], a[4:7], v[24:27], v[184:187]\n\t"
    "v_cvt_pk_bf16_f32 v192, v192, v193\n\t"
    "v_cvt_pk_bf16_f32 v193, v194, v195\n\t"
    "v_mfma_f32_16x16x32_bf16 v[188:191], a[4:7], v[28:31], v[188:191]\n\t"
    "v_cvt_pk_bf16_f32 v194, v196, v197\n\t"
    "v_cvt_pk_bf16_f32 v195, v198, v199\n\t"
    "s_waitcnt vmcnt(30)\n\t"
    "v_mfma_f32_16x16x32_bf16 v[176:179], a[8:11], v[32:35], v[176:179]\n\t"
    "v_cvt_pk_bf16_f32 v200, v200, v201\n\t"
    "v_cvt_pk_bf16_f32 v201, v202, v203\n\t"
    "buffer_load_dwordx4 a[116:119], %[voff], s[36:39], s56 offen offset:1024 sc1\n\t"
    "v_mfma_f32_16x16x32_bf16 v[180:183], a[8:11], v[36:39], v[180:183]\n\t"
    "v_cvt_pk_bf16_f32 v202, v204, v205\n\t"
    "v_cvt_pk_bf16_f32 v203, v206, v207\n\t"
    "v_mfma_f32_16x16x32_bf16 v[184:187], a[8:11], v[40:43], v[184:187]\n\t"
    "buffer_store_dwordx4 v[192:195], %[voff], s[44:47], s60 offen offset:0 sc1\n\t"
    "v_mfma_f32_16x16x32_bf16 v[188:191], a[8:11], v[44:47], v[188:191]\n\t"
    "s_waitcnt vmcnt(30)\n\t"
    "v_mfma_f32_16x16x32_bf16 v[176:179], a[12:15], v[48:51], v[176:179]\n\t"
    "buffer_load_dwordx4 a[120:123], %[voff], s[36:39], s56 offen offset:2048 sc1\n\t"
    "v_mfma_f32_16x16x32_bf16 v[180:183], a[12:15], v[52:55], v[180:183]\n\t"
    "v_mfma_f32_16x16x32_bf16 v[184:187], a[12:15], v[56:59], v[184:187]\n\t"
    "v_mfma_f32_16x16x32_bf16 v[188:191], a[12:15], v[60:63], v[188:191]\n\t"
    "s_waitcnt vmcnt(30)\n\t"
    "v_mfma_f32_16x16x32_bf16 v[176:179], a[16:19], v[64:67], v[176:179]\n\t"
    "buffer_load_dwordx4 a[124:127], %[voff], s[36:39], s56 offen offset:3072 sc1\n\t"
    "v_mfma_f32_16x16x32_bf16 v[180:183], a[16:19], v[68:71], v[180:183]\n\t"
    "s_add_u32 s56, s56, 4096\n\t"
    "v_mfma_f32_16x16x32_bf16 v[184:187], a[16:19], v[72:75], v[184:187]\n\t"
    "v_mfma_f32_16x16x32_bf16 v[188:191], a[16:19], v[76:79], v[188:191]\n\t"
    "s_waitcnt vmcnt(30)\n\t"
    "v_mfma_f32_16x16x32_bf16 v[176:179], a[20:23], v[80:83], v[176:179]\n\t"
    "buffer_load_dwordx4 a[128:131], %[voff], s[36:39], s56 offen offset:0 sc1\n\t"
    "v_mfma_f32_16x16x32_bf16 v[180:183], a[20:23], v[84:87], v[180:183]\n\t"
    "v_mfma_f32_16x16x32_bf16 v[184:187], a[20:23], v[88:91], v[184:187]\n\t"
    "v_mfma_f32_16x16x32_bf16 v[188:191], a[20:23], v[92:95], v[188:191]\n\t"
    "s_waitcnt vmcnt(30)\n\t"
    "v_mfma_f32_16x16x32_bf16 v[176:179], a[24:27], v[96:99], v[176:179]\n\t"
    "v_mfma_f32_16x16x32_bf16 v[180:183], a[24:27], v[100:103], v[180:183]\n\t"
    "s_waitcnt vmcnt(14)\n\t"
    "s_mov_b64 exec, 1\n\t"
    "s_add_u32 s61, %[fs], 4\n\t"
    "s_and_b32 s61, s61, 7\n\t"
    "s_lshl_b32 s61, s61, 7\n\t"
    "s_add_u32 s61, s61, %[w32]\n\t"
    "buffer_atomic_add v210, off, s[52:55], s61 offset:0\n\t"
    "s_mov_b64 exec, -1\n\t"
    "v_mfma_f32_16x16x32_bf16 v[184:187], a[24:27], v[104:107], v[184:187]\n\t"
    "v_mfma_f32_16x16x32_bf16 v[188:191], a[24:27], v[108:111], v[188:191]\n\t"
    "s_waitcnt vmcnt(30)\n\t"
    "v_mfma_f32_16x16x32_bf16 v[176:179], a[28:31], v[112:115], v[176:179]\n\t"
    "buffer_load_dwordx4 a[132:135], %[voff], s[36:39], s56 offen offset:1024 sc1\n\t"
    "v_mfma_f32_16x16x32_bf16 v[180:183], a[28:31], v[116:119], v[180:183]\n\t"
    "v_mfma_f32_16x16x32_bf16 v[184:187], a[28:31], v[120:123], v[184:187]\n\t"
    "v_mfma_f32_16x16x32_bf16 v[188:191], a[28:31], v[124:127], v[188:191]\n\t"
    "s_waitcnt vmcnt(30)\n\t"
    "v_mfma_f32_16x16x32_bf16 v[176:179], a[32:35], v[128:131], v[176:179]\n\t"
    "buffer_load_dwordx4 a[136:139], %[voff], s[36:39], s56 offen offset:2048 sc1\n\t"
    "v_mfma_f32_16x16x32_bf16 v[180:183], a[32:35], v[132:135], v[180:183]\n\t"
    "v_mfma_f32_16x16x32_bf16 v[184:187], a[32:35], v[136:139], v[184:187]\n\t"
    "v_mfma_f32_16x16x32_bf16 v[188:191], a[32:35], v[140:143], v[188:191]\n\t"
    "s_waitcnt vmcnt(30)\n\t"
    "v_mfma_f32_16x16x32_bf16 v[176:179], a[36:39], v[144:147], v[176:179]\n\t"
    "buffer_load_dwordx4 a[140:143], %[voff], s[36:39], s56 offen offset:3072 sc1\n\t"
    "v_mfma_f32_16x16x32_bf16 v[180:183], a[36:39], v[148:151], v[180:183]\n\t"
    "buffer_store_dwordx4 v[200:203], %[voff], s[44:47], s60 offen offset:1024 sc1\n\t"
    "v_mfma_f32_16x16x32_bf16 v[184:187], a[36:39], v[152:155], v[184:187]\n\t"
    "s_add_u32 s56, s56, 4096\n\t"
    "v_mfma_f32_16x16x32_bf16 v[188:191], a[36:39], v[156:159], v[188:191]\n\t"
    "s_waitcnt vmcnt(31)\n\t"
    "v_mfma_f32_16x16x32_bf16 v[176:179], a[40:43], v[160:163], v[176:179]\n\t"
    "buffer_load_dwordx4 a[0:3], %[voff], s[36:39], s56 offen offset:0 sc1\n\t"
    "v_mfma_f32_16x16x32_bf16 v[180:183], a[40:43], v[164:167], v[180:183]\n\t"
    "v_mfma_f32_16x16x32_bf16 v[184:187], a[40:43], v[168:171], v[184:187]\n\t"
    "v_mfma_f32_16x16x32_bf16 v[188:191], a[40:43], v[172:175], v[188:191]\n\t"
    "s_waitcnt vmcnt(31)\n\t"
    "v_mfma_f32_16x16x32_bf16 v[176:179], a[44:47], a[144:147], v[176:179]\n\t"
    "buffer_load_dwordx4 a[4:7], %[voff], s[36:39], s56 offen offset:1024 sc1\n\t"
    "v_mfma_f32_16x16x32_bf16 v[180:183], a[44:47], a[148:151], v[180:183]\n\t"
    "v_mfma_f32_16x16x32_bf16 v[184:187], a[44:47], a[152:155], v[184:187]\n\t"
    "v_mfma_f32_16x16x32_bf16 v[188:191], a[44:47], a[156:159], v[188:191]\n\t"
    "s_waitcnt vmcnt(31)\n\t"
    "v_mfma_f32_16x16x32_bf16 v[176:179], a[48:51], a[160:163], v[176:179]\n\t"
    "buffer_load_dwordx4 a[8:11], %[voff], s[36:39], s56 offen offset:2048 sc1\n\t"
    "v_mfma_f32_16x16x32_bf16 v[180:183], a[48:51], a[164:167], v[180:183]\n\t"
    "v_mfma_f32_16x16x32_bf16 v[184:187], a[48:51], a[168:171], v[184:187]\n\t"
    "v_mfma_f32_16x16x32_bf16 v[188:191], a[48:51], a[172:175], v[188:191]\n\t"
    "s_waitcnt vmcnt(30)\n\t"
    "v_mfma_f32_16x16x32_bf16 v[176:179], a[52:55], a[176:179], v[176:179]\n\t"
    "buffer_load_dwordx4 a[12:15], %[voff], s[36:39], s56 offen offset:3072 sc1\n\t"
    "v_mfma_f32_16x16x32_bf16 v[180:183], a[52:55], a[180:183], v[180:183]\n\t"
    "s_add_u32 s56, s56, 4096\n\t"
    "v_mfma_f32_16x16x32_bf16 v[184:187], a[52:55], a[184:187], v[184:187]\n\t"
    "v_mfma_f32_16x16x32_bf16 v[188:191], a[52:55], a[188:191], v[188:191]\n\t"
    "s_waitcnt vmcnt(30)\n\t"
    "v_mfma_f32_16x16x32_bf16 v[176:179], a[56:59], a[192:195], v[176:179]\n\t"
    "buffer_load_dwordx4 a[16:19], %[voff], s[36:39], s56 offen offset:0 sc1\n\t"
    "v_mfma_f32_16x16x32_bf16 v[180:183], a[56:59], a[196:199], v[180:183]\n\t"
    "v_mfma_f32_16x16x32_bf16 v[184:187], a[56:59], a[200:203], v[184:187]\n\t"
    "v_mfma_f32_16x16x32_bf16 v[188:191], a[56:59], a[204:207], v[188:191]\n\t"
    "s_waitcnt vmcnt(30)\n\t"
    "v_mfma_f32_16x16x32_bf16 v[176:179], a[60:63], a[208:211], v[176:179]\n\t"
    "buffer_load_dwordx4 a[20:23], %[voff], s[36:39], s56 offen offset:1024 sc1\n\t"
    "v_mfma_f32_16x16x32_bf16 v[180:183], a[60:63], a[212:215], v[180:183]\n\t"
    "v_mfma_f32_16x16x32_bf16 v[184:187], a[60:63], a[216:219], v[184:187]\n\t"
    "v_mfma_f32_16x16x32_bf16 v[188:191], a[60:63], a[220:223], v[188:191]\n\t"
    "s_waitcnt vmcnt(30)\n\t"
    "v_mfma_f32_16x16x32_bf16 v[176:179], a[64:67], a[224:227], v[176:179]\n\t"
    "buffer_load_dwordx4 a[24:27], %[voff], s[36:39], s56 offen offset:2048 sc1\n\t"
    "v_mfma_f32_16x16x32_bf16 v[180:183], a[64:67], a[228:231], v[180:183]\n\t"
    "v_mfma_f32_16x16x32_bf16 v[184:187], a[64:67], a[232:235], v[184:187]\n\t"
    "v_mfma_f32_16x16x32_bf16 v[188:191], a[64:67], a[236:239], v[188:191]\n\t"
    "s_waitcnt vmcnt(29)\n\t"
    "v_mfma_f32_16x16x32_bf16 v[176:179], a[68:71], a[240:243], v[176:179]\n\t"
    "buffer_load_dwordx4 a[28:31], %[voff], s[36:39], s56 offen offset:3072 sc1\n\t"
    "v_mfma_f32_16x16x32_bf16 v[180:183], a[68:71], a[244:247], v[180:183]\n\t"
    "s_add_u32 s56, s56, 4096\n\t"
    "v_mfma_f32_16x16x32_bf16 v[184:187], a[68:71], a[248:251], v[184:187]\n\t"
    "v_mfma_f32_16x16x32_bf16 v[188:191], a[68:71], a[252:255], v[188:191]\n\t"
    "s_waitcnt vmcnt(29)\n\t"
    "v_mfma_f32_16x16x32_bf16 v[192:195], a[72:75], v[0:3], 0\n\t"
    "v_mfma_f32_16x16x32_bf16 v[196:199], a[72:75], v[4:7], 0\n\t"
    "v_mfma_f32_16x16x32_bf16 v[200:203], a[72:75], v[8:11], 0\n\t"
    "v_mfma_f32_16x16x32_bf16 v[204:207], a[72:75], v[12:15], 0\n\t"
    "s_waitcnt vmcnt(28)\n\t"
    "v_mfma_f32_16x16x32_bf16 v[192:195], a[76:79], v[16:19], v[192:195]\n\t"
    "s_add_u32 s60, %[fs], 6\n\t"
    "s_and_b32 s60, s60, 7\n\t"
    "s_lshl_b32 s60, s60, 13\n\t"
    "s_add_u32 s60, s60, %[w2k]\n\t"
    "s_add_u32 s60, s60, 12582912\n\t"
    "buffer_load_dwordx4 a[32:35], %[voff], s[36:39], s56 offen offset:0 sc1\n\t"
    "v_mfma_f32_16x16x32_bf16 v[196:199], a[76:79], v[20:23], v[196:199]\n\t"
    "v_mfma_f32_16x16x32_bf16 v[200:203], a[76:79], v[24:27], v[200:203]\n\t"
    "v_cvt_pk_bf16_f32 v176, v176, v177\n\t"
    "v_cvt_pk_bf16_f32 v177, v178, v179\n\t"
    "v_mfma_f32_16x16x32_bf16 v[204:207], a[76:79], v[28:31], v[204:207]\n\t"
    "v_cvt_pk_bf16_f32 v178, v180, v181\n\t"
    "v_cvt_pk_bf16_f32 v179, v182, v183\n\t"
    "s_waitcnt vmcnt(27)\n\t"
    "v_mfma_f32_16x16x32_bf16 v[192:195], a[80:83], v[32:35], v[192:195]\n\t"
    "v_cvt_pk_bf16_f32 v184, v184, v185\n\t"
    "v_cvt_pk_bf16_f32 v185, v186, v187\n\t"
    "buffer_load_dwordx4 a[36:39], %[voff], s[36:39], s56 offen offset:1024 sc1\n\t"
    "v_mfma_f32_16x16x32_bf16 v[196:199], a[80:83], v[36:39], v[196:199]\n\t"
    "v_cvt_pk_bf16_f32 v186, v188, v189\n\t"
    "v_cvt_pk_bf16_f32 v187, v190, v191\n\t"
    "buffer_load_dword v213, off, s[52:55], %[cb] offset:0 sc1\n\t"
    "v_mfma_f32_16x16x32_bf16 v[200:203], a[80:83], v[40:43], v[200:203]\n\t"
    "buffer_store_dwordx4 v[176:179], %[voff], s[44:47], s60 offen offset:0 sc1\n\t"
    "v_mfma_f32_16x16x32_bf16 v[204:207], a[80:83], v[44:47], v[204:207]\n\t"
    "s_waitcnt vmcnt(29)\n\t"
    "v_mfma_f32_16x16x32_bf16 v[192:195], a[84:87], v[48:51], v[192:195]\n\t"
    "buffer_load_dwordx4 a[40:43], %[voff], s[36:39], s56 offen offset:2048 sc1\n\t"
    "v_mfma_f32_16x16x32_bf16 v[196:199], a[84:87], v[52:55], v[196:199]\n\t"
    "v_mfma_f32_16x16x32_bf16 v[200:203], a[84:87], v[56:59], v[200:203]\n\t"
    "v_mfma_f32_16x16x32_bf16 v[204:207], a[84:87], v[60:63], v[204:207]\n\t"
    "s_waitcnt vmcnt(29)\n\t"
    "v_mfma_f32_16x16x32_bf16 v[192:195], a[88:91], v[64:67], v[192:195]\n\t"
    "buffer_load_dwordx4 a[44:47], %[voff], s[36:39], s56 offen offset:3072 sc1\n\t"
    "v_mfma_f32_16x16x32_bf16 v[196:199], a[88:91], v[68:71], v[196:199]\n\t"
    "s_add_u32 s56, s56, 4096\n\t"
    "v_mfma_f32_16x16x32_bf16 v[200:203], a[88:91], v[72:75], v[200:203]\n\t"
    "v_mfma_f32_16x16x32_bf16 v[204:207], a[88:91], v[76:79], v[204:207]\n\t"
    "s_waitcnt vmcnt(29)\n\t"
    "v_mfma_f32_16x16x32_bf16 v[192:195], a[92:95], v[80:83], v[192:195]\n\t"
    "buffer_load_dwordx4 a[48:51], %[voff], s[36:39], s56 offen offset:0 sc1\n\t"
    "v_mfma_f32_16x16x32_bf16 v[196:199], a[92:95], v[84:87], v[196:199]\n\t"
    "v_mfma_f32_16x16x32_bf16 v[200:203], a[92:95], v[88:91], v[200:203]\n\t"
    "v_mfma_f32_16x16x32_bf16 v[204:207], a[92:95], v[92:95], v[204:207]\n\t"
    "s_waitcnt vmcnt(29)\n\t"
    "v_mfma_f32_16x16x32_bf16 v[192:195], a[96:99], v[96:99], v[192:195]\n\t"
    "buffer_load_dwordx4 a[52:55], %[voff], s[36:39], s56 offen offset:1024 sc1\n\t"
    "v_mfma_f32_16x16x32_bf16 v[196:199], a[96:99], v[100:103], v[196:199]\n\t"
    "s_waitcnt vmcnt(16)\n\t"
    "s_mov_b64 exec, 1\n\t"
    "s_add_u32 s61, %[fs], 5\n\t"
    "s_and_b32 s61, s61, 7\n\t"
    "s_lshl_b32 s61, s61, 7\n\t"
    "s_add_u32 s61, s61, %[w32]\n\t"
    "buffer_atomic_add v210, off, s[52:55], s61 offset:4\n\t"
    "s_mov_b64 exec, -1\n\t"
    "v_mfma_f32_16x16x32_bf16 v[200:203], a[96:99], v[104:107], v[200:203]\n\t"
    "v_mfma_f32_16x16x32_bf16 v[204:207], a[96:99], v[108:111], v[204:207]\n\t"
    "s_waitcnt vmcnt(30)\n\t"
    "v_mfma_f32_16x16x32_bf16 v[192:195], a[100:103], v[112:115], v[192:195]\n\t"
    "buffer_load_dwordx4 a[56:59], %[voff], s[36:39], s56 offen offset:2048 sc1\n\t"
    "v_mfma_f32_16x16x32_bf16 v[196:199], a[100:103], v[116:119], v[196:199]\n\t"
    "v_mfma_f32_16x16x32_bf16 v[200:203], a[100:103], v[120:123], v[200:203]\n\t"
    "v_mfma_f32_16x16x32_bf16 v[204:207], a[100:103], v[124:127], v[204:207]\n\t"
    "s_waitcnt vmcnt(30)\n\t"
    "v_mfma_f32_16x16x32_bf16 v[192:195], a[104:107], v[128:131], v[192:195]\n\t"
    "buffer_load_dwordx4 a[60:63], %[voff], s[36:39], s56 offen offset:3072 sc1\n\t"
    "v_mfma_f32_16x16x32_bf16 v[196:199], a[104:107], v[132:135], v[196:199]\n\t"
    "s_add_u32 s56, s56, 4096\n\t"
    "v_mfma_f32_16x16x32_bf16 v[200:203], a[104:107], v[136:139], v[200:203]\n\t"
    "v_mfma_f32_16x16x32_bf16 v[204:207], a[104:107], v[140:143], v[204:207]\n\t"
    "s_waitcnt vmcnt(30)\n\t"
    "v_mfma_f32_16x16x32_bf16 v[192:195], a[108:111], v[144:147], v[192:195]\n\t"
    "buffer_load_dwordx4 a[64:67], %[voff], s[36:39], s56 offen offset:0 sc1\n\t"
    "v_mfma_f32_16x16x32_bf16 v[196:199], a[108:111], v[148:151], v[196:199]\n\t"
    "buffer_store_dwordx4 v[184:187], %[voff], s[44:47], s60 offen offset:1024 sc1\n\t"
    "v_mfma_f32_16x16x32_bf16 v[200:203], a[108:111], v[152:155], v[200:203]\n\t"
    "v_mfma_f32_16x16x32_bf16 v[204:207], a[108:111], v[156:159], v[204:207]\n\t"
    "s_waitcnt vmcnt(31)\n\t"
    "v_mfma_f32_16x16x32_bf16 v[192:195], a[112:115], v[160:163], v[192:195]\n\t"
    "buffer_load_dwordx4 a[68:71], %[voff], s[36:39], s56 offen offset:1024 sc1\n\t"
    "v_mfma_f32_16x16x32_bf16 v[196:199], a[112:115], v[164:167], v[196:199]\n\t"
    "v_mfma_f32_16x16x32_bf16 v[200:203], a[112:115], v[168:171], v[200:203]\n\t"
    "v_mfma_f32_16x16x32_bf16 v[204:207], a[112:115], v[172:175], v[204:207]\n\t"
    "s_waitcnt vmcnt(31)\n\t"
    "v_mfma_f32_16x16x32_bf16 v[192:195], a[116:119], a[144:147], v[192:195]\n\t"
    "v_mfma_f32_16x16x32_bf16 v[196:199], a[116:119], a[148:151], v[196:199]\n\t"
    "v_mfma_f32_16x16x32_bf16 v[200:203], a[116:119], a[152:155], v[200:203]\n\t"
    "v_mfma_f32_16x16x32_bf16 v[204:207], a[116:119], a[156:159], v[204:207]\n\t"
    "s_waitcnt vmcnt(29)\n\t"
    "v_mfma_f32_16x16x32_bf16 v[192:195], a[120:123], a[160:163], v[192:195]\n\t"
    "v_mfma_f32_16x16x32_bf16 v[196:199], a[120:123], a[164:167], v[196:199]\n\t"
    "v_mfma_f32_16x16x32_bf16 v[200:203], a[120:123], a[168:171], v[200:203]\n\t"
    "v_mfma_f32_16x16x32_bf16 v[204:207], a[120:123], a[172:175], v[204:207]\n\t"
    "s_waitcnt vmcnt(28)\n\t"
    "v_mfma_f32_16x16x32_bf16 v[192:195], a[124:127], a[176:179], v[192:195]\n\t"
    "v_mfma_f32_16x16x32_bf16 v[196:199], a[124:127], a[180:183], v[196:199]\n\t"
    "v_mfma_f32_16x16x32_bf16 v[200:203], a[124:127], a[184:187], v[200:203]\n\t"
    "v_mfma_f32_16x16x32_bf16 v[204:207], a[124:127], a[188:191], v[204:207]\n\t"
    "s_waitcnt vmcnt(27)\n\t"
    "v_mfma_f32_16x16x32_bf16 v[192:195], a[128:131], a[192:195], v[192:195]\n\t"
    "v_mfma_f32_16x16x32_bf16 v[196:199], a[128:131], a[196:199], v[196:199]\n\t"
    "v_mfma_f32_16x16x32_bf16 v[200:203], a[128:131], a[200:203], v[200:203]\n\t"
    "v_mfma_f32_16x16x32_bf16 v[204:207], a[128:131], a[204:207], v[204:207]\n\t"
    "s_waitcnt vmcnt(25)\n\t"
    "v_mfma_f32_16x16x32_bf16 v[192:195], a[132:135], a[208:211], v[192:195]\n\t"
    "v_mfma_f32_16x16x32_bf16 v[196:199], a[132:135], a[212:215], v[196:199]\n\t"
    "v_mfma_f32_16x16x32_bf16 v[200:203], a[132:135], a[216:219], v[200:203]\n\t"
    "v_mfma_f32_16x16x32_bf16 v[204:207], a[132:135], a[220:223], v[204:207]\n\t"
    "s_waitcnt vmcnt(24)\n\t"
    "v_mfma_f32_16x16x32_bf16 v[192:195], a[136:139], a[224:227], v[192:195]\n\t"
    "v_mfma_f32_16x16x32_bf16 v[196:199], a[136:139], a[228:231], v[196:199]\n\t"
    "v_mfma_f32_16x16x32_bf16 v[200:203], a[136:139], a[232:235], v[200:203]\n\t"
    "v_mfma_f32_16x16x32_bf16 v[204:207], a[136:139], a[236:239], v[204:207]\n\t"
    "s_waitcnt vmcnt(23)\n\t"
    "v_mfma_f32_16x16x32_bf16 v[192:195], a[140:143], a[240:243], v[192:195]\n\t"
    "v_mfma_f32_16x16x32_bf16 v[196:199], a[140:143], a[244:247], v[196:199]\n\t"
    "s_waitcnt vmcnt(11)\n\t"
    "s_movk_i32 s61, 32767\n\t"
    "L_q0_%=:\n\t"
    "v_readfirstlane_b32 s57, v213\n\t"
    "s_cmp_ge_u32 s57, 12\n\t"
    "s_cbranch_scc1 L_q0ok_%=\n\t"
    "s_sub_u32 s61, s61, 1\n\t"
    "s_cmp_eq_u32 s61, 0\n\t"
    "s_cbranch_scc1 L_q0ok_%=\n\t"
    "buffer_load_dword v213, off, s[52:55], %[cb] offset:0 sc1\n\t"
    "s_waitcnt vmcnt(0)\n\t"
    "s_branch L_q0_%=\n\t"
    "L_q0ok_%=:\n\t"
    "v_mfma_f32_16x16x32_bf16 v[200:203], a[140:143], a[248:251], v[200:203]\n\t"
    "v_mfma_f32_16x16x32_bf16 v[204:207], a[140:143], a[252:255], v[204:207]\n\t"
    "s_waitcnt vmcnt(21)\n\t"
    "v_mfma_f32_16x16x32_bf16 v[176:179], a[0:3], v[0:3], 0\n\t"
    "v_mfma_f32_16x16x32_bf16 v[180:183], a[0:3], v[4:7], 0\n\t"
    "s_add_u32 s59, %[rb], 2097152\n\t"
    "buffer_load_dwordx4 a[72:75], %[voff], s[44:47], s59 offen offset:0 sc1\n\t"
    "buffer_load_dwordx4 a[76:79], %[voff], s[44:47], s59 offen offset:1024 sc1\n\t"
    "v_mfma_f32_16x16x32_bf16 v[184:187], a[0:3], v[8:11], 0\n\t"
    "v_mfma_f32_16x16x32_bf16 v[188:191], a[0:3], v[12:15], 0\n\t"
    "s_waitcnt vmcnt(22)\n\t"
    "v_mfma_f32_16x16x32_bf16 v[176:179], a[4:7], v[16:19], v[176:179]\n\t"
    "s_add_u32 s60, %[fs], 7\n\t"
    "s_and_b32 s60, s60, 7\n\t"
    "s_lshl_b32 s60, s60, 13\n\t"
    "s_add_u32 s60, s60, %[w2k]\n\t"
    "s_add_u32 s60, s60, 14680064\n\t"
    "v_mfma_f32_16x16x32_bf16 v[180:183], a[4:7], v[20:23], v[180:183]\n\t"
    "s_add_u32 s59, %[rb], 4194304\n\t"
    "buffer_load_dwordx4 a[80:83], %[voff], s[44:47], s59 offen offset:0 sc1\n\t"
    "buffer_load_dwordx4 a[84:87], %[voff], s[44:47], s59 offen offset:1024 sc1\n\t"
    "v_mfma_f32_16x16x32_bf16 v[184:187], a[4:7], v[24:27], v[184:187]\n\t"
    "v_cvt_pk_bf16_f32 v192, v192, v193\n\t"
    "v_cvt_pk_bf16_f32 v193, v194, v195\n\t"
    "v_mfma_f32_16x16x32_bf16 v[188:191], a[4:7], v[28:31], v[188:191]\n\t"
    "v_cvt_pk_bf16_f32 v194, v196, v197\n\t"
    "v_cvt_pk_bf16_f32 v195, v198, v199\n\t"
    "s_waitcnt vmcnt(23)\n\t"
    "v_mfma_f32_16x16x32_bf16 v[176:179], a[8:11], v[32:35], v[176:179]\n\t"
    "v_cvt_pk_bf16_f32 v200, v200, v201\n\t"
    "v_cvt_pk_bf16_f32 v201, v202, v203\n\t"
    "v_mfma_f32_16x16x32_bf16 v[180:183], a[8:11], v[36:39], v[180:183]\n\t"
    "v_cvt_pk_bf16_f32 v202, v204, v205\n\t"
    "v_cvt_pk_bf16_f32 v203, v206, v207\n\t"
    "v_mfma_f32_16x16x32_bf16 v[184:187], a[8:11], v[40:43], v[184:187]\n\t"
    "buffer_store_dwordx4 v[192:195], %[voff], s[44:47], s60 offen offset:0 sc1\n\t"
    "v_mfma_f32_16x16x32_bf16 v[188:191], a[8:11], v[44:47], v[188:191]\n\t"
    "s_waitcnt vmcnt(23)\n\t"
    "v_mfma_f32_16x16x32_bf16 v[176:179], a[12:15], v[48:51], v[176:179]\n\t"
    "v_mfma_f32_16x16x32_bf16 v[180:183], a[12:15], v[52:55], v[180:183]\n\t"
    "buffer_store_dwordx4 v[200:203], %[voff], s[44:47], s60 offen offset:1024 sc1\n\t"
    "v_mfma_f32_16x16x32_bf16 v[184:187], a[12:15], v[56:59], v[184:187]\n\t"
    "v_mfma_f32_16x16x32_bf16 v[188:191], a[12:15], v[60:63], v[188:191]\n\t"
    "s_waitcnt vmcnt(23)\n\t"
    "v_mfma_f32_16x16x32_bf16 v[176:179], a[16:19], v[64:67], v[176:179]\n\t"
    "v_mfma_f32_16x16x32_bf16 v[180:183], a[16:19], v[68:71], v[180:183]\n\t"
    "s_waitcnt vmcnt(7)\n\t"
    "s_mov_b64 exec, 1\n\t"
    "s_add_u32 s61, %[fs], 6\n\t"
    "s_and_b32 s61, s61, 7\n\t"
    "s_lshl_b32 s61, s61, 7\n\t"
    "s_add_u32 s61, s61, %[w32]\n\t"
    "buffer_atomic_add v210, off, s[52:55], s61 offset:4\n\t"
    "s_mov_b64 exec, -1\n\t"
    "v_mfma_f32_16x16x32_bf16 v[184:187], a[16:19], v[72:75], v[184:187]\n\t"
    "s_add_u32 s59, %[rb], 6291456\n\t"
    "buffer_load_dwordx4 a[88:91], %[voff], s[44:47], s59 offen offset:0 sc1\n\t"
    "buffer_load_dwordx4 a[92:95], %[voff], s[44:47], s59 offen offset:1024 sc1\n\t"
    "v_mfma_f32_16x16x32_bf16 v[188:191], a[16:19], v[76:79], v[188:191]\n\t"
    "s_waitcnt vmcnt(25)\n\t"
    "v_mfma_f32_16x16x32_bf16 v[176:179], a[20:23], v[80:83], v[176:179]\n\t"
    "ds_read_b64_tr_b16 v[32:33], %[sa0]\n\t"
    "v_mfma_f32_16x16x32_bf16 v[180:183], a[20:23], v[84:87], v[180:183]\n\t"
    "ds_read_b32 v40, %[srs]\n\t"
    "v_mfma_f32_16x16x32_bf16 v[184:187], a[20:23], v[88:91], v[184:187]\n\t"
    "s_add_u32 s59, %[rb], 8388608\n\t"
    "buffer_load_dwordx4 a[96:99], %[voff], s[44:47], s59 offen offset:0 sc1\n\t"
    "buffer_load_dwordx4 a[100:103], %[voff], s[44:47], s59 offen offset:1024 sc1\n\t"
    "v_mfma_f32_16x16x32_bf16 v[188:191], a[20:23], v[92:95], v[188:191]\n\t"
    "s_waitcnt vmcnt(26)\n\t"
    "v_mfma_f32_16x16x32_bf16 v[176:179], a[24:27], v[96:99], v[176:179]\n\t"
    "ds_read_b64_tr_b16 v[0:1], %[ra0]\n\t"
    "v_mfma_f32_16x16x32_bf16 v[180:183], a[24:27], v[100:103], v[180:183]\n\t"
    "ds_read_b64_tr_b16 v[4:5], %[ra1]\n\t"
    "v_mfma_f32_16x16x32_bf16 v[184:187], a[24:27], v[104:107], v[184:187]\n\t"
    "ds_read_b64_tr_b16 v[8:9], %[ra2]\n\t"
    "v_mfma_f32_16x16x32_bf16 v[188:191], a[24:27], v[108:111], v[188:191]\n\t"
    "ds_read_b64_tr_b16 v[12:13], %[ra3]\n\t"
    "s_waitcnt vmcnt(25)\n\t"
    "v_mfma_f32_16x16x32_bf16 v[176:179], a[28:31], v[112:115], v[176:179]\n\t"
    "ds_read_b32 v16, %[rsr] offset:0\n\t"
    "v_mfma_f32_16x16x32_bf16 v[180:183], a[28:31], v[116:119], v[180:183]\n\t"
    "ds_read_b32 v17, %[rsr] offset:64\n\t"
    "v_mfma_f32_16x16x32_bf16 v[184:187], a[28:31], v[120:123], v[184:187]\n\t"
    "s_lshl_b32 s59, %[fs], 13\n\t"
    "s_add_u32 s61, s59, 4096\n\t"
    "buffer_load_dwordx4 a[104:107], %[shv], s[44:47], s59 offen sc1\n\t"
    "buffer_load_dwordx4 a[108:111], %[shv], s[44:47], s61 offen sc1\n\t"
    "ds_read_b32 v18, %[rsr] offset:128\n\t"
    "v_mfma_f32_16x16x32_bf16 v[188:191], a[28:31], v[124:127], v[188:191]\n\t"
    "ds_read_b32 v19, %[rsr] offset:192\n\t"
    "s_waitcnt vmcnt(26)\n\t"
    "v_mfma_f32_16x16x32_bf16 v[176:179], a[32:35], v[128:131], v[176:179]\n\t"
    "v_mfma_f32_16x16x32_bf16 v[180:183], a[32:35], v[132:135], v[180:183]\n\t"
    "v_mfma_f32_16x16x32_bf16 v[184:187], a[32:35], v[136:139], v[184:187]\n\t"
    "v_mfma_f32_16x16x32_bf16 v[188:191], a[32:35], v[140:143], v[188:191]\n\t"
    "s_waitcnt vmcnt(25)\n\t"
    "v_mfma_f32_16x16x32_bf16 v[176:179], a[36:39], v[144:147], v[176:179]\n\t"
    "v_mfma_f32_16x16x32_bf16 v[180:183], a[36:39], v[148:151], v[180:183]\n\t"
    "v_mfma_f32_16x16x32_bf16 v[184:187], a[36:39], v[152:155], v[184:187]\n\t"
    "v_mfma_f32_16x16x32_bf16 v[188:191], a[36:39], v[156:159], v[188:191]\n\t"
    "s_waitcnt vmcnt(22)\n\t"
    "v_mfma_f32_16x16x32_bf16 v[176:179], a[40:43], v[160:163], v[176:179]\n\t"
    "v_mfma_f32_16x16x32_bf16 v[180:183], a[40:43], v[164:167], v[180:183]\n\t"
    "v_mfma_f32_16x16x32_bf16 v[184:187], a[40:43], v[168:171], v[184:187]\n\t"
    "v_mfma_f32_16x16x32_bf16 v[188:191], a[40:43], v[172:175], v[188:191]\n\t"
    "s_waitcnt vmcnt(21)\n\t"
    "v_mfma_f32_16x16x32_bf16 v[176:179], a[44:47], a[144:147], v[176:179]\n\t"
    "s_waitcnt lgkmcnt(0)\n\t"
    "v_and_b32 v3, 0xffff0000, v1\n\t"
    "v_lshlrev_b32 v2, 16, v1\n\t"
    "v_and_b32 v1, 0xffff0000, v0\n\t"
    "v_lshlrev_b32 v0, 16, v0\n\t"
    "v_mfma_f32_16x16x32_bf16 v[180:183], a[44:47], a[148:151], v[180:183]\n\t"
    "v_mfma_f32_16x16x32_bf16 v[184:187], a[44:47], a[152:155], v[184:187]\n\t"
    "v_mfma_f32_16x16x32_bf16 v[188:191], a[44:47], a[156:159], v[188:191]\n\t"
    "s_waitcnt vmcnt(20)\n\t"
    "v_mfma_f32_16x16x32_bf16 v[176:179], a[48:51], a[160:163], v[176:179]\n\t"
    "v_and_b32 v7, 0xffff0000, v5\n\t"
    "v_lshlrev_b32 v6, 16, v5\n\t"
    "v_and_b32 v5, 0xffff0000, v4\n\t"
    "v_lshlrev_b32 v4, 16, v4\n\t"
    "v_mfma_f32_16x16x32_bf16 v[180:183], a[48:51], a[164:167], v[180:183]\n\t"
    "v_mfma_f32_16x16x32_bf16 v[184:187], a[48:51], a[168:171], v[184:187]\n\t"
    "v_mfma_f32_16x16x32_bf16 v[188:191], a[48:51], a[172:175], v[188:191]\n\t"
    "s_waitcnt vmcnt(19)\n\t"
    "v_mfma_f32_16x16x32_bf16 v[176:179], a[52:55], a[176:179], v[176:179]\n\t"
    "v_and_b32 v11, 0xffff0000, v9\n\t"
    "v_lshlrev_b32 v10, 16, v9\n\t"
    "v_and_b32 v9, 0xffff0000, v8\n\t"
    "v_lshlrev_b32 v8, 16, v8\n\t"
    "v_mfma_f32_16x16x32_bf16 v[180:183], a[52:55], a[180:183], v[180:183]\n\t"
    "s_waitcnt vmcnt(7)\n\t"
    "s_mov_b64 exec, 1\n\t"
    "s_add_u32 s61, %[fs], 7\n\t"
    "s_and_b32 s61, s61, 7\n\t"
    "s_lshl_b32 s61, s61, 7\n\t"
    "s_add_u32 s61, s61, %[w32]\n\t"
    "buffer_atomic_add v210, off, s[52:55], s61 offset:4\n\t"
    "s_mov_b64 exec, -1\n\t"
    "v_mfma_f32_16x16x32_bf16 v[184:187], a[52:55], a[184:187], v[184:187]\n\t"
    "v_mfma_f32_16x16x32_bf16 v[188:191], a[52:55], a[188:191], v[188:191]\n\t"
    "s_waitcnt vmcnt(18)\n\t"
    "v_mfma_f32_16x16x32_bf16 v[176:179], a[56:59], a[192:195], v[176:179]\n\t"
    "v_and_b32 v15, 0xffff0000, v13\n\t"
    "v_lshlrev_b32 v14, 16, v13\n\t"
    "v_and_b32 v13, 0xffff0000, v12\n\t"
    "v_lshlrev_b32 v12, 16, v12\n\t"
    "v_mfma_f32_16x16x32_bf16 v[180:183], a[56:59], a[196:199], v[180:183]\n\t"
    "v_mfma_f32_16x16x32_bf16 v[184:187], a[56:59], a[200:203], v[184:187]\n\t"
    "v_mfma_f32_16x16x32_bf16 v[188:191], a[56:59], a[204:207], v[188:191]\n\t"
    "s_waitcnt vmcnt(17)\n\t"
    "v_mfma_f32_16x16x32_bf16 v[176:179], a[60:63], a[208:211], v[176:179]\n\t"
    "v_and_b32 v35, 0xffff0000, v33\n\t"
    "v_lshlrev_b32 v34, 16, v33\n\t"
    "v_and_b32 v33, 0xffff0000, v32\n\t"
    "v_lshlrev_b32 v32, 16, v32\n\t"
    "v_mfma_f32_16x16x32_bf16 v[180:183], a[60:63], a[212:215], v[180:183]\n\t"
    "v_mfma_f32_16x16x32_bf16 v[184:187], a[60:63], a[216:219], v[184:187]\n\t"
    "v_mfma_f32_16x16x32_bf16 v[188:191], a[60:63], a[220:223], v[188:191]\n\t"
    "s_waitcnt vmcnt(16)\n\t"
    "v_mfma_f32_16x16x32_bf16 v[176:179], a[64:67], a[224:227], v[176:179]\n\t"
    "v_mfma_f32_16x16x32_bf16 v[180:183], a[64:67], a[228:231], v[180:183]\n\t"
    "v_mfma_f32_16x16x32_bf16 v[184:187], a[64:67], a[232:235], v[184:187]\n\t"
    "v_mfma_f32_16x16x32_bf16 v[188:191], a[64:67], a[236:239], v[188:191]\n\t"
    "s_waitcnt vmcnt(14)\n\t"
    "v_mfma_f32_16x16x32_bf16 v[176:179], a[68:71], a[240:243], v[176:179]\n\t"
    "v_mfma_f32_16x16x32_bf16 v[180:183], a[68:71], a[244:247], v[180:183]\n\t"
    "v_mfma_f32_16x16x32_bf16 v[184:187], a[68:71], a[248:251], v[184:187]\n\t"
    "v_mfma_f32_16x16x32_bf16 v[188:191], a[68:71], a[252:255], v[188:191]\n\t"
    "s_mov_b64 s[40:41], %[pO]\n\t"
    "s_mov_b32 s42, 73728\n\t"
    "buffer_load_dword v213, off, s[52:55], %[cb] offset:4 sc1\n\t"
    "s_waitcnt vmcnt(14)\n\t"
    "v_mfma_f32_16x16x32_bf16 v[176:179], %[sel0], a[72:75], v[176:179]\n\t"
    "v_mfma_f32_16x16x32_bf16 v[180:183], %[sel1], a[72:75], v[180:183]\n\t"
    "s_waitcnt vmcnt(13)\n\t"
    "v_mfma_f32_16x16x32_bf16 v[184:187], %[sel0], a[76:79], v[184:187]\n\t"
    "v_mfma_f32_16x16x32_bf16 v[188:191], %[sel1], a[76:79], v[188:191]\n\t"
    "s_waitcnt vmcnt(12)\n\t"
    "v_mfma_f32_16x16x32_bf16 v[176:179], %[sel0], a[80:83], v[176:179]\n\t"
    "v_mfma_f32_16x16x32_bf16 v[180:183], %[sel1], a[80:83], v[180:183]\n\t"
    "s_waitcnt vmcnt(11)\n\t"
    "v_mfma_f32_16x16x32_bf16 v[184:187], %[sel0], a[84:87], v[184:187]\n\t"
    "v_mfma_f32_16x16x32_bf16 v[188:191], %[sel1], a[84:87], v[188:191]\n\t"
    "s_waitcnt vmcnt(7)\n\t"
    "v_mfma_f32_16x16x32_bf16 v[176:179], %[sel0], a[88:91], v[176:179]\n\t"
    "v_mfma_f32_16x16x32_bf16 v[180:183], %[sel1], a[88:91], v[180:183]\n\t"
    "s_waitcnt vmcnt(6)\n\t"
    "v_mfma_f32_16x16x32_bf16 v[184:187], %[sel0], a[92:95], v[184:187]\n\t"
    "v_mfma_f32_16x16x32_bf16 v[188:191], %[sel1], a[92:95], v[188:191]\n\t"
    "s_waitcnt vmcnt(5)\n\t"
    "v_mfma_f32_16x16x32_bf16 v[176:179], %[sel0], a[96:99], v[176:179]\n\t"
    "v_mfma_f32_16x16x32_bf16 v[180:183], %[sel1], a[96:99], v[180:183]\n\t"
    "s_waitcnt vmcnt(4)\n\t"
    "v_mfma_f32_16x16x32_bf16 v[184:187], %[sel0], a[100:103], v[184:187]\n\t"
    "v_mfma_f32_16x16x32_bf16 v[188:191], %[sel1], a[100:103], v[188:191]\n\t"
    "s_waitcnt vmcnt(3)\n\t"
    "v_mfma_f32_16x16x32_bf16 v[24:27], %[sel2], a[104:107], 0\n\t"
    "s_waitcnt vmcnt(2)\n\t"
    "v_mfma_f32_16x16x32_bf16 v[24:27], %[sel2], a[108:111], v[24:27]\n\t"
    "s_waitcnt vmcnt(0)\n\t"
    "s_movk_i32 s61, 32767\n\t"
    "L_q1_%=:\n\t"
    "v_readfirstlane_b32 s57, v213\n\t"
    "s_cmp_ge_u32 s57, 3\n\t"
    "s_cbranch_scc1 L_q1ok_%=\n\t"
    "s_sub_u32 s61, s61, 1\n\t"
    "s_cmp_eq_u32 s61, 0\n\t"
    "s_cbranch_scc1 L_q1ok_%=\n\t"
    "buffer_load_dword v213, off, s[52:55], %[cb] offset:4 sc1\n\t"
    "s_waitcnt vmcnt(0)\n\t"
    "s_branch L_q1_%=\n\t"
    "L_q1ok_%=:\n\t"
    "s_add_u32 s59, %[rb], 10485760\n\t"
    "buffer_load_dwordx4 v[48:51], %[voff], s[44:47], s59 offen offset:0 sc1\n\t"
    "buffer_load_dwordx4 v[52:55], %[voff], s[44:47], s59 offen offset:1024 sc1\n\t"
    "s_add_u32 s59, %[rb], 12582912\n\t"
    "buffer_load_dwordx4 v[56:59], %[voff], s[44:47], s59 offen offset:0 sc1\n\t"
    "buffer_load_dwordx4 v[60:63], %[voff], s[44:47], s59 offen offset:1024 sc1\n\t"
    "s_add_u32 s59, %[rb], 14680064\n\t"
    "buffer_load_dwordx4 v[64:67], %[voff], s[44:47], s59 offen offset:0 sc1\n\t"
    "buffer_load_dwordx4 v[68:71], %[voff], s[44:47], s59 offen offset:1024 sc1\n\t"
    "v_fmac_f32 v32, v40, v24\n\t"
    "v_fmac_f32 v33, v40, v25\n\t"
    "v_fmac_f32 v34, v40, v26\n\t"
    "v_fmac_f32 v35, v40, v27\n\t"
    "v_cvt_pk_bf16_f32 v32, v32, v33\n\t"
    "v_cvt_pk_bf16_f32 v33, v34, v35\n\t"
    "s_waitcnt vmcnt(5)\n\t"
    "v_mfma_f32_16x16x32_bf16 v[176:179], %[sel0], v[48:51], v[176:179]\n\t"
    "v_mfma_f32_16x16x32_bf16 v[180:183], %[sel1], v[48:51], v[180:183]\n\t"
    "s_waitcnt vmcnt(4)\n\t"
    "v_mfma_f32_16x16x32_bf16 v[184:187], %[sel0], v[52:55], v[184:187]\n\t"
    "v_mfma_f32_16x16x32_bf16 v[188:191], %[sel1], v[52:55], v[188:191]\n\t"
    "s_waitcnt vmcnt(3)\n\t"
    "v_mfma_f32_16x16x32_bf16 v[176:179], %[sel0], v[56:59], v[176:179]\n\t"
    "v_mfma_f32_16x16x32_bf16 v[180:183], %[sel1], v[56:59], v[180:183]\n\t"
    "s_waitcnt vmcnt(2)\n\t"
    "v_mfma_f32_16x16x32_bf16 v[184:187], %[sel0], v[60:63], v[184:187]\n\t"
    "v_mfma_f32_16x16x32_bf16 v[188:191], %[sel1], v[60:63], v[188:191]\n\t"
    "s_waitcnt vmcnt(1)\n\t"
    "v_mfma_f32_16x16x32_bf16 v[176:179], %[sel0], v[64:67], v[176:179]\n\t"
    "v_mfma_f32_16x16x32_bf16 v[180:183], %[sel1], v[64:67], v[180:183]\n\t"
    "s_waitcnt vmcnt(0)\n\t"
    "v_mfma_f32_16x16x32_bf16 v[184:187], %[sel0], v[68:71], v[184:187]\n\t"
    "v_mfma_f32_16x16x32_bf16 v[188:191], %[sel1], v[68:71], v[188:191]\n\t"
    "s_nop 7\n\t"
    "s_mov_b32 s61, 0\n\t"
    "v_fmac_f32 v0, v16, v176\n\t"
    "v_fmac_f32 v1, v16, v177\n\t"
    "v_fmac_f32 v2, v16, v178\n\t"
    "v_fmac_f32 v3, v16, v179\n\t"
    "v_cvt_pk_bf16_f32 v0, v0, v1\n\t"
    "v_cvt_pk_bf16_f32 v1, v2, v3\n\t"
    "buffer_store_dwordx2 v[0:1], %[oo], s[40:43], s61 offen\n\t"
    "s_add_u32 s61, s61, 18432\n\t"
    "v_fmac_f32 v4, v17, v180\n\t"
    "v_fmac_f32 v5, v17, v181\n\t"
    "v_fmac_f32 v6, v17, v182\n\t"
    "v_fmac_f32 v7, v17, v183\n\t"
    "v_cvt_pk_bf16_f32 v4, v4, v5\n\t"
    "v_cvt_pk_bf16_f32 v5, v6, v7\n\t"
    "buffer_store_dwordx2 v[4:5], %[oo], s[40:43], s61 offen\n\t"
    "s_add_u32 s61, s61, 18432\n\t"
    "v_fmac_f32 v8, v18, v184\n\t"
    "v_fmac_f32 v9, v18, v185\n\t"
    "v_fmac_f32 v10, v18, v186\n\t"
    "v_fmac_f32 v11, v18, v187\n\t"
    "v_cvt_pk_bf16_f32 v8, v8, v9\n\t"
    "v_cvt_pk_bf16_f32 v9, v10, v11\n\t"
    "buffer_store_dwordx2 v[8:9], %[oo], s[40:43], s61 offen\n\t"
    "s_add_u32 s61, s61, 18432\n\t"
    "v_fmac_f32 v12, v19, v188\n\t"
    "v_fmac_f32 v13, v19, v189\n\t"
    "v_fmac_f32 v14, v19, v190\n\t"
    "v_fmac_f32 v15, v19, v191\n\t"
    "v_cvt_pk_bf16_f32 v12, v12, v13\n\t"
    "v_cvt_pk_bf16_f32 v13, v14, v15\n\t"
    "buffer_store_dwordx2 v[12:13], %[oo], s[40:43], s61 offen\n\t"
    "buffer_store_dwordx2 v[32:33], %[so], s[40:43], 0 offen\n\t"
    "s_waitcnt vmcnt(0) lgkmcnt(0)\n\t"
    : [hb0] "+v"(hb[0]), [hb1] "+v"(hb[1]), [hb2] "+v"(hb[2]), [hb3] "+v"(hb[3]), [hwr] "+v"(hwr)
    : [voff] "v"(voff), [Hw0] "v"(Hw[0]), [Hw1] "v"(Hw[1]), [Hw2] "v"(Hw[2]), [Hw3] "v"(Hw[3]), [stw] "v"(stw), [csr] "v"(csr), [csb] "s"(csb), [hrb] "s"(hrb),
      [p4] "v"(p4), [rso] "v"(rso), [rsr] "v"(rsr), [srs] "v"(srs), [oo] "v"(oo), [so] "v"(so),
      [ra0] "v"(ra[0]), [ra1] "v"(ra[1]), [ra2] "v"(ra[2]), [ra3] "v"(ra[3]), [sa0] "v"(sa0), [shv] "v"(shv), [sel2] "v"(sel2), [sel0] "v"(sel0), [sel1] "v"(sel1),
      [pW] "s"(pW), [pH] "s"(pH), [pP] "s"(pP), [pC] "s"(pC), [pR] "s"(pR), [pO] "s"(pO), [pG] "s"(pG), [gws] "s"(gwo), [gho] "v"(gho), [tag] "s"(tag), [wok] "s"(wok),
      [wid] "s"(w), [fs] "s"(fs), [w2k] "s"(w2k), [w32] "s"(w32), [cb] "s"(cb), [rb] "s"(rb)
    : "v0", "v1", "v2", "v3", "v4", "v5", "v6", "v7", "v8", "v9", "v10", "v11", "v12", "v13", "v14", "v15", "v16", "v17", "v18", "v19", "v20", "v21", "v22", "v23",
      "v24", "v25", "v26", "v27", "v28", "v29", "v30", "v31", "v32", "v33", "v34", "v35", "v36", "v37", "v38", "v39", "v40", "v41", "v42", "v43", "v44", "v45", "v46", "v47",
      "v48", "v49", "v50", "v51", "v52", "v53", "v54", "v55", "v56", "v57", "v58", "v59", "v60", "v61", "v62", "v63", "v64", "v65", "v66", "v67", "v68", "v69", "v70", "v71",
      "v72", "v73", "v74", "v75", "v76", "v77", "v78", "v79", "v80", "v81", "v82", "v83", "v84", "v85", "v86", "v87", "v88", "v89", "v90", "v91", "v92", "v93", "v94", "v95",
      "v96", "v97", "v98", "v99", "v100", "v101", "v102", "v103", "v104", "v105", "v106", "v107", "v108", "v109", "v110", "v111", "v112", "v113", "v114", "v115", "v116", "v117", "v118", "v119",
      "v120", "v121", "v122", "v123", "v124", "v125", "v126", "v127", "v128", "v129", "v130", "v131", "v132", "v133", "v134", "v135", "v136", "v137", "v138", "v139", "v140", "v141", "v142", "v143",
      "v144", "v145", "v146", "v147", "v148", "v149", "v150", "v151", "v152", "v153", "v154", "v155", "v156", "v157", "v158", "v159", "v160", "v161", "v162", "v163", "v164", "v165", "v166", "v167",
      "v168", "v169", "v170", "v171", "v172", "v173", "v174", "v175", "v176", "v177", "v178", "v179", "v180", "v181", "v182", "v183", "v184", "v185", "v186", "v187", "v188", "v189", "v190", "v191",
      "v192", "v193", "v194", "v195", "v196", "v197", "v198", "v199", "v200", "v201", "v202", "v203", "v204", "v205", "v206", "v207", "v208", "v209", "v210", "v211", "v212", "v213", "v214", "v215",
      "a0", "a1", "a2", "a3", "a4", "a5", "a6", "a7", "a8", "a9", "a10", "a11", "a12", "a13", "a14", "a15", "a16", "a17", "a18", "a19", "a20", "a21", "a22", "a23",
      "a24", "a25", "a26", "a27", "a28", "a29", "a30", "a31", "a32", "a33", "a34", "a35", "a36", "a37", "a38", "a39", "a40", "a41", "a42", "a43", "a44", "a45", "a46", "a47",
      "a48", "a49", "a50", "a51", "a52", "a53", "a54", "a55", "a56", "a57", "a58", "a59", "a60", "a61", "a62", "a63", "a64", "a65", "a66", "a67", "a68", "a69", "a70", "a71",
      "a72", "a73", "a74", "a75", "a76", "a77", "a78", "a79", "a80", "a81", "a82", "a83", "a84", "a85", "a86", "a87", "a88", "a89", "a90", "a91", "a92", "a93", "a94", "a95",
      "a96", "a97", "a98", "a99", "a100", "a101", "a102", "a103", "a104", "a105", "a106", "a107", "a108", "a109", "a110", "a111", "a112", "a113", "a114", "a115", "a116", "a117", "a118", "a119",
      "a120", "a121", "a122", "a123", "a124", "a125", "a126", "a127", "a128", "a129", "a130", "a131", "a132", "a133", "a134", "a135", "a136", "a137", "a138", "a139", "a140", "a141", "a142", "a143",
      "a144", "a145", "a146", "a147", "a148", "a149", "a150", "a151", "a152", "a153", "a154", "a155", "a156", "a157", "a158", "a159", "a160", "a161", "a162", "a163", "a164", "a165", "a166", "a167",
      "a168", "a169", "a170", "a171", "a172", "a173", "a174", "a175", "a176", "a177", "a178", "a179", "a180", "a181", "a182", "a183", "a184", "a185", "a186", "a187", "a188", "a189", "a190", "a191",
      "a192", "a193", "a194", "a195", "a196", "a197", "a198", "a199", "a200", "a201", "a202", "a203", "a204", "a205", "a206", "a207", "a208", "a209", "a210", "a211", "a212", "a213", "a214", "a215",
      "a216", "a217", "a218", "a219", "a220", "a221", "a222", "a223", "a224", "a225", "a226", "a227", "a228", "a229", "a230", "a231", "a232", "a233", "a234", "a235", "a236", "a237", "a238", "a239",
      "a240", "a241", "a242", "a243", "a244", "a245", "a246", "a247", "a248", "a249", "a250", "a251", "a252", "a253", "a254", "a255", "s36", "s37", "s38", "s39", "s40", "s41", "s42", "s43",
      "s44", "s45", "s46", "s47", "s48", "s49", "s50", "s51", "s52", "s53", "s54", "s55", "s56", "s57", "s58", "s59", "s60", "s61", "s62", "s63", "vcc", "scc", "memory");
    asm volatile("code_end_lbl:");
}

extern "C" void kernel_launch(void* const* d_in, const int* in_sizes, int n_in,
                              void* d_out, int out_size, void* d_ws, size_t ws_size,
                              hipStream_t stream) {
    const u16* q = (const u16*)d_in[0];
    const u16* k = (const u16*)d_in[1];
    const u16* v = (const u16*)d_in[2];
    const u16* shk = (const u16*)d_in[3];
    const u16* filt = (const u16*)d_in[4];
    const u16* Dv = (const u16*)d_in[5];
    const u16* w1 = (const u16*)d_in[7];
    const u16* w2 = (const u16*)d_in[8];
    u16* out = (u16*)d_out;
    char* ws = (char*)d_ws;
    char* XT = ws;
    char* QT = ws + (3 << 20);
    u16* hT = (u16*)(ws + (8 << 20));
    float* csum = (float*)(ws + (12 << 20));
    char* wst = ws + (16 << 20);
    char* part = ws + (32 << 20);
    unsigned* cnt = (unsigned*)(ws + (13 << 20));
    kk_ffn<<<256, 256, 0, stream>>>(q, k, v, shk, Dv, filt, XT, QT, w1, w2, hT, (u32x4*)wst, csum, part, cnt, out);
}
```
